```python
import torch

def _softmax(x, axis=-1):
    return torch.softmax(x, dim=axis)
def _gelu(x):
    return torch.nn.functional.gelu(x, approximate="tanh")
def _silu(x):
    return torch.nn.functional.silu(x)
def _sigmoid(x):
    return torch.sigmoid(x)
def _layernorm(x, axis=-1, eps=1e-5):
    m = x.mean(dim=axis, keepdim=True)
    v = x.var(dim=axis, keepdim=True, unbiased=False)
    return (x - m) / torch.sqrt(v + eps)
def _scatter_add(tgt, idx, src, axis=0):
    return tgt.clone().index_add_(axis, idx, src)
def _segment_sum(src, seg, n):
    out = torch.zeros((n,) + src.shape[1:], dtype=src.dtype)
    return out.index_add_(0, seg, src)
def _pad(x, pw):
    flat = tuple(v for pair in reversed(pw) for v in pair)
    return torch.nn.functional.pad(x, flat)

kernel_name = 'recurrent_scan__gqa_block_cached__mixer_token_mlp__decode'

def setup_inputs():

    _w = (lambda a: (lambda t: t.to(torch.bfloat16) if t.is_floating_point() else t)(torch.as_tensor(a)))
    def _A_setup():
        B, Sq, D = 8, 1, 640
        Hq, Hkv, Sk, Dh = 32, 32, 4096, 128
        assert Hq % Hkv == 0, (Hq, Hkv)
        return {
            "x": _w((lambda *s: torch.randn(*s, dtype=torch.bfloat16) * 0.4)(B, Sq, D)),
            "k_cache": _w((lambda *s: torch.randn(*s, dtype=torch.bfloat16) * 1.6)(B, Hkv, Sk, Dh)),
            "v_cache": _w((lambda *s: torch.randn(*s, dtype=torch.bfloat16) * 1.6)(B, Hkv, Sk, Dh)),
            "wq": _w((lambda *s: torch.randn(*s, dtype=torch.bfloat16) * 1.6)(D, Hq * Dh) * (D ** -0.5)),
            "wo": _w((lambda *s: torch.randn(*s, dtype=torch.bfloat16) * 1.6)(Hq * Dh, D) * ((Hq * Dh) ** -0.5)),
        }
    _A_in = _A_setup()

    def _B_setup():
        B, S, D, F = 8, 1, 640, 1728
        return {
            "x": _w((lambda *s: torch.randn(*s, dtype=torch.bfloat16) * 0.4)(B, S, D)),
            "w1": _w((lambda *s: torch.randn(*s, dtype=torch.bfloat16) * 1.6)(S, F) * (S ** -0.5)),
            "b1": _w((lambda *s: torch.randn(*s, dtype=torch.bfloat16) * 1.6)(F) * 0.02),
            "w2": _w((lambda *s: torch.randn(*s, dtype=torch.bfloat16) * 1.6)(F, S) * (F ** -0.5)),
            "b2": _w((lambda *s: torch.randn(*s, dtype=torch.bfloat16) * 1.6)(S) * 0.02),
        }
    _B_in = _B_setup()

    return {
        "A_x": _A_in["x"],
        "A_k_cache": _A_in["k_cache"],
        "A_v_cache": _A_in["v_cache"],
        "A_wq": _A_in["wq"],
        "A_wo": _A_in["wo"],
        "B_x": _B_in["x"],
        "B_w1": _B_in["w1"],
        "B_b1": _B_in["b1"],
        "B_w2": _B_in["w2"],
        "B_b2": _B_in["b2"],
    }

def reference(A_x, A_k_cache, A_v_cache, A_wq, A_wo, B_x, B_w1, B_b1, B_w2, B_b2):
    def _A_ref(x, k_cache, v_cache, wq, wo):
        B, Sq, D = x.shape
        Hq, Dh = 32, 128
        Hkv = k_cache.shape[1]
        rep = Hq // Hkv
        q = (lambda x, *d: x.permute(*d))((x @ wq).reshape(B, Sq, Hq, Dh), 0, 2, 1, 3)
        k = (lambda x, n, axis: torch.repeat_interleave(x, n, dim=axis))(k_cache, rep, axis=1)
        v = (lambda x, n, axis: torch.repeat_interleave(x, n, dim=axis))(v_cache, rep, axis=1)
        scores = torch.einsum("bhqd,bhkd->bhqk", q, k) * (Dh ** -0.5)
        p = _softmax(scores, axis=-1)
        o = torch.einsum("bhqk,bhkd->bhqd", p, v)
        return (lambda x, *d: x.permute(*d))(o, 0, 2, 1, 3).reshape(B, Sq, Hq * Dh) @ wo

    def _B_ref(x, w1, b1, w2, b2):
        B, S, D = x.shape
        h = _layernorm(x, axis=-1)
        u = torch.einsum("bsd,sf->bdf", h, w1) + b1
        u = _gelu(u)
        y = torch.einsum("bdf,fs->bds", u, w2) + b2
        return (lambda x, *d: x.permute(*d))(y, 0, 2, 1)

    x = A_x
    xn = _layernorm(x, axis=-1)
    s = _A_ref(xn, A_k_cache, A_v_cache, A_wq, A_wo)
    g = _silu(_B_ref(xn, B_w1, B_b1, B_w2, B_b2))
    return x + s * g
```

```cpp
#include <hip/hip_runtime.h>
#include <hip/hip_bf16.h>
#include <stdint.h>

#define GAS __attribute__((address_space(1)))
#define LAS __attribute__((address_space(3)))
typedef unsigned v4u __attribute__((ext_vector_type(4)));
typedef unsigned v2u __attribute__((ext_vector_type(2)));
typedef float v4f __attribute__((ext_vector_type(4)));
typedef float v2f __attribute__((ext_vector_type(2)));
typedef __bf16 bf16x8 __attribute__((ext_vector_type(8)));
typedef __bf16 bf16x2 __attribute__((ext_vector_type(2)));

#define EMU_SCORE 1

constexpr int NB = 8, DM = 640, NH = 32, SK = 4096, DH = 128, FF = 1728;
constexpr int COLS = DM / NH;
constexpr float LOG2E = 1.4426950408889634f;
constexpr float SCALE = 0.08838834764831845f;
constexpr unsigned SPIN_CAP = 1u << 20;

constexpr size_t WS_GRAN = 4096;

__device__ __forceinline__ v4f mfma16(v4u a, v4u b, v4f c) {
  return __builtin_amdgcn_mfma_f32_16x16x32_bf16(__builtin_bit_cast(bf16x8, a), __builtin_bit_cast(bf16x8, b), c, 0, 0, 0);
}
__device__ __forceinline__ unsigned cvtpk(float lo, float hi) {
  bf16x2 r = {(__bf16)lo, (__bf16)hi};
  return __builtin_bit_cast(unsigned, r);
}
__device__ __forceinline__ float bflo(unsigned u) { return __builtin_bit_cast(float, u << 16); }
__device__ __forceinline__ float bfhi(unsigned u) { return __builtin_bit_cast(float, u & 0xffff0000u); }
__device__ __forceinline__ float rbf(float x) { return (float)(__bf16)x; }
__device__ __forceinline__ v4u ldnt(const void* p) { return __builtin_nontemporal_load((const GAS v4u*)p); }
__device__ __forceinline__ v4u ldg(const void* p) { return *(const GAS v4u*)p; }

template <int CTRL> __device__ __forceinline__ float dpp(float x) {
  return __builtin_bit_cast(float, __builtin_amdgcn_mov_dpp(__builtin_bit_cast(int, x), CTRL, 0xf, 0xf, true));
}
constexpr int XOR1 = 0xB1, XOR2 = 0x4E, XOR7 = 0x141, XOR15 = 0x140;
__device__ __forceinline__ float swap16_sum(float x) {
  auto s = __builtin_amdgcn_permlane16_swap(__float_as_uint(x), __float_as_uint(x), false, false);
  return __uint_as_float(s[0]) + __uint_as_float(s[1]);
}
__device__ __forceinline__ float swap32_sum(float x) {
  auto s = __builtin_amdgcn_permlane32_swap(__float_as_uint(x), __float_as_uint(x), false, false);
  return __uint_as_float(s[0]) + __uint_as_float(s[1]);
}
__device__ __forceinline__ float swap16_max(float x) {
  auto s = __builtin_amdgcn_permlane16_swap(__float_as_uint(x), __float_as_uint(x), false, false);
  return fmaxf(__uint_as_float(s[0]), __uint_as_float(s[1]));
}
__device__ __forceinline__ float swap32_max(float x) {
  auto s = __builtin_amdgcn_permlane32_swap(__float_as_uint(x), __float_as_uint(x), false, false);
  return fmaxf(__uint_as_float(s[0]), __uint_as_float(s[1]));
}
__device__ __forceinline__ float row16_sum(float x) {
  x += dpp<XOR1>(x); x += dpp<XOR2>(x); x += dpp<XOR7>(x); x += dpp<XOR15>(x); return x;
}
__device__ __forceinline__ float half32_sum(float x) { return swap16_sum(row16_sum(x)); }
__device__ __forceinline__ float wave_sum(float x) { return swap32_sum(half32_sum(x)); }

__device__ __forceinline__ void wg_barrier() {
  asm volatile("s_waitcnt lgkmcnt(0)" ::: "memory");
  __builtin_amdgcn_s_barrier();
  asm volatile("" ::: "memory");
}

__device__ __forceinline__ void ln_stats(const float (&v)[16], bool has2, float& m, float& den) {
  float s = 0.f;
#pragma unroll
  for (int i = 0; i < 16; ++i) s += v[i];
  s = wave_sum(s);
  const float mean = s * (1.0f / DM);
  float q = 0.f;
#pragma unroll
  for (int i = 0; i < 8; ++i) { const float d = v[i] - mean; q = fmaf(d, d, q); }
  float q2 = 0.f;
#pragma unroll
  for (int i = 8; i < 16; ++i) { const float d = v[i] - mean; q2 = fmaf(d, d, q2); }
  q += has2 ? q2 : 0.f;
  q = wave_sum(q);
  const float var = rbf(q * (1.0f / DM));
  m = rbf(mean);
  den = rbf(__builtin_sqrtf(rbf(var + 1e-5f)));
}
__device__ __forceinline__ float ln_apply(float x, float m, float den) { return rbf(rbf(x - m) / den); }

struct Smem {
  float qpart[4][DH];
  unsigned qbf[4][64];
  float comb[4][DH + 4];
  float spart[4][DM];
  float gpart[4][32];
  unsigned tag[4];
};

__global__ void __launch_bounds__(512, 2)
fused_decode(const uint16_t* __restrict__ x, const char* __restrict__ kc, const char* __restrict__ vc,
             const char* __restrict__ wq, const char* __restrict__ wo, const uint16_t* __restrict__ w1,
             const uint16_t* __restrict__ b1, const uint16_t* __restrict__ w2, const uint16_t* __restrict__ b2,
             uint16_t* __restrict__ out, char* ws) {
  __shared__ __attribute__((aligned(16))) Smem sm;
  const int tid = threadIdx.x, lane = tid & 63, wave = __builtin_amdgcn_readfirstlane(tid >> 6);
  const int bid = blockIdx.x;
  const int h = (bid & 7) * 4 + ((bid >> 3) & 3), b = bid >> 5;
  const int t = lane >> 4, c = lane & 15;
  v4u dmask;
#pragma unroll
  for (int i = 0; i < 4; ++i) dmask[i] = (c >> 1) == i ? ((c & 1) ? 0xffff0000u : 0x0000ffffu) : 0u;
  auto diag = [&](unsigned dup) { v4u a; a[0] = dup & dmask[0]; a[1] = dup & dmask[1]; a[2] = dup & dmask[2]; a[3] = dup & dmask[3]; return a; };

  GAS unsigned* epoch_p = (GAS unsigned*)(ws + b * 256);

  if (wave < 4) {
    constexpr int NSTEP = SK / (4 * 32);
    const size_t hb = ((size_t)(b * NH + h)) * (SK * DH * 2);
    const char* kb = kc + hb + wave * 8192 + (4 * (c & 3) + (c >> 2)) * 256 + t * 16;
    const char* vb = vc + hb + wave * 8192 + lane * 16;
    v4u Kr[2][8], Vr[2][8];
    auto issueK = [&](int slot, int st) {
      const char* p = kb + (size_t)st * 32768;
#pragma unroll
      for (int u = 0; u < 2; ++u)
#pragma unroll
        for (int s = 0; s < 4; ++s) Kr[slot][u * 4 + s] = ldnt(p + u * 4096 + s * 64);
    };
    auto issueV = [&](int slot, int st) {
      const char* p = vb + (size_t)st * 32768;
#pragma unroll
      for (int i = 0; i < 8; ++i) Vr[slot][i] = ldnt(p + i * 1024);
    };
    issueK(0, 0); issueV(0, 0); issueK(1, 1); issueV(1, 1);
    __builtin_amdgcn_sched_barrier(0);
    wg_barrier();
    v4u qf[4];
    {
      float q0 = 0.f, q1 = 0.f;
#pragma unroll
      for (int j = 0; j < 4; ++j) { const v2f p = *(const v2f*)&sm.qpart[j][2 * lane]; q0 += p[0]; q1 += p[1]; }
      sm.qbf[wave][lane] = cvtpk(q0, q1);
      asm volatile("s_waitcnt lgkmcnt(0)" ::: "memory");
#pragma unroll
      for (int s = 0; s < 4; ++s) qf[s] = *(const v4u*)&sm.qbf[wave][16 * s + 4 * t];
    }
    float m = -INFINITY, l = 0.f;
    v4f O0 = {0.f, 0.f, 0.f, 0.f}, O1 = {0.f, 0.f, 0.f, 0.f};
    auto step = [&](int slot, int st, bool more) {
      v4f S0 = {0.f, 0.f, 0.f, 0.f}, S1 = {0.f, 0.f, 0.f, 0.f};
#pragma unroll
      for (int s = 0; s < 4; ++s) { S0 = mfma16(Kr[slot][s], qf[s], S0); S1 = mfma16(Kr[slot][4 + s], qf[s], S1); }
      if (more) issueK(slot, st + 2);
      float sc[8];
#pragma unroll
      for (int i = 0; i < 4; i += 2) {
        unsigned a = cvtpk(S0[i], S0[i + 1]), d = cvtpk(S1[i], S1[i + 1]);
        a = cvtpk(bflo(a) * SCALE, bfhi(a) * SCALE); d = cvtpk(bflo(d) * SCALE, bfhi(d) * SCALE);
        sc[i] = bflo(a); sc[i + 1] = bfhi(a); sc[4 + i] = bflo(d); sc[5 + i] = bfhi(d);
      }
      constexpr float C2 = LOG2E;
      float mx = fmaxf(fmaxf(fmaxf(sc[0], sc[1]), fmaxf(sc[2], sc[3])), fmaxf(fmaxf(sc[4], sc[5]), fmaxf(sc[6], sc[7])));
      mx = swap32_max(swap16_max(mx));
      const float mn = fmaxf(m, mx);
      const float alpha = __builtin_amdgcn_exp2f((m - mn) * C2);
      const float mo = mn * C2;
      m = mn;
      float p[8], ps = 0.f;
#pragma unroll
      for (int i = 0; i < 8; ++i) { p[i] = __builtin_amdgcn_exp2f(fmaf(sc[i], C2, -mo)); ps += p[i]; }
      l = fmaf(l, alpha, ps);
      O0 *= alpha; O1 *= alpha;
#pragma unroll
      for (int i = 0; i < 4; ++i) {
        O0 = mfma16(diag(cvtpk(p[i], p[i])), Vr[slot][i], O0);
        O1 = mfma16(diag(cvtpk(p[4 + i], p[4 + i])), Vr[slot][4 + i], O1);
      }
      if (more) issueV(slot, st + 2);
      __builtin_amdgcn_sched_barrier(0);
    };
    for (int st = 0; st < NSTEP - 2; st += 2) { step(0, st, true); step(1, st + 1, true); }
    step(0, NSTEP - 2, false); step(1, NSTEP - 1, false);
    O0 += O1;
    l = swap32_sum(swap16_sum(l));
    const float mlog = m * LOG2E;
    if (lane < 32) *(v4f*)&sm.comb[wave][8 * c + 4 * t] = O0;
    if (lane == 0) { sm.comb[wave][DH] = mlog; sm.comb[wave][DH + 1] = l; }
    wg_barrier();
  } else {
    const int j = wave - 4;
    const uint16_t* xr = x + b * DM;
    const v4u xa = ldg(xr + 8 * lane);
    v4u xb2 = {0u, 0u, 0u, 0u};
    if (lane < 16) xb2 = ldg(xr + 512 + 8 * lane);
    const int rot = b % 5;
    v4u xq[5];
    int rr[5];
#pragma unroll
    for (int n = 0; n < 5; ++n) { int r = n + rot; r = r >= 5 ? r - 5 : r; rr[n] = r; xq[n] = ldg(xr + 160 * j + 40 * t + 8 * r); }
    const unsigned tag0 = __hip_atomic_load(epoch_p, __ATOMIC_RELAXED, __HIP_MEMORY_SCOPE_AGENT) + 1u;
    v4u W[40];
    {
      const char* p = wq + (size_t)(160 * j + 40 * t) * (NH * DH * 2) + h * (DH * 2) + c * 16;
#pragma unroll
      for (int n = 0; n < 5; ++n)
#pragma unroll
        for (int i = 0; i < 8; ++i) W[n * 8 + i] = ldg(p + (size_t)(8 * rr[n] + i) * (NH * DH * 2));
    }
    __builtin_amdgcn_sched_barrier(0);
    float xv[16];
#pragma unroll
    for (int i = 0; i < 4; ++i) { xv[2 * i] = bflo(xa[i]); xv[2 * i + 1] = bfhi(xa[i]); xv[8 + 2 * i] = bflo(xb2[i]); xv[9 + 2 * i] = bfhi(xb2[i]); }
    float m1, den1;
    ln_stats(xv, lane < 16, m1, den1);
    {
      v4f Q0 = {0.f, 0.f, 0.f, 0.f}, Q1 = {0.f, 0.f, 0.f, 0.f};
#pragma unroll
      for (int n = 0; n < 5; ++n)
#pragma unroll
        for (int i = 0; i < 4; ++i) {
          const float e0 = ln_apply(bflo(xq[n][i]), m1, den1), e1 = ln_apply(bfhi(xq[n][i]), m1, den1);
          Q0 = mfma16(diag(cvtpk(e0, e0)), W[n * 8 + 2 * i], Q0);
          Q1 = mfma16(diag(cvtpk(e1, e1)), W[n * 8 + 2 * i + 1], Q1);
        }
      Q0 += Q1;
      if (lane < 32) *(v4f*)&sm.qpart[j][8 * c + 4 * t] = Q0;
    }
    wg_barrier();
    float gw1[7], gb1[7], gw2[7];
    unsigned xcol;
    {
      unsigned r1[7], r2[7], r3[7];
#pragma unroll
      for (int i = 0; i < 7; ++i) {
        const int f = 256 * i + 64 * (i == 6 && j == 3 ? 0 : j) + lane;
        r1[i] = w1[f]; r2[i] = b1[f]; r3[i] = w2[f];
      }
      xcol = xr[COLS * h + (lane < COLS ? lane : 0)];
      r3[6] = j == 3 ? 0u : r3[6];
#pragma unroll
      for (int i = 0; i < 7; ++i) { gw1[i] = bflo(r1[i]); gb1[i] = bflo(r2[i]); gw2[i] = bflo(r3[i]); }
    }
    {
      const char* p = wo + (size_t)(DH * h + 32 * j + 8 * t) * (DM * 2) + c * 16;
#pragma unroll
      for (int rg = 0; rg < 8; ++rg)
#pragma unroll
        for (int cb = 0; cb < 5; ++cb) W[rg * 5 + cb] = ldg(p + rg * (DM * 2) + cb * 256);
    }
    __builtin_amdgcn_sched_barrier(0);
    float xn[16];
#pragma unroll
    for (int i = 0; i < 16; ++i) xn[i] = ln_apply(xv[i], m1, den1);
#pragma unroll
    for (int i = 8; i < 16; ++i) xn[i] = lane < 16 ? xn[i] : 0.f;
    float m2, den2;
    ln_stats(xn, lane < 16, m2, den2);
    const float hv = ln_apply(ln_apply(bflo(xcol), m1, den1), m2, den2);
    float gsum = 0.f;
#pragma unroll 4
    for (int col = 0; col < COLS; ++col) {
      const float hc = __builtin_bit_cast(float, __builtin_amdgcn_readlane(__builtin_bit_cast(int, hv), col));
      float acc = 0.f;
#pragma unroll
      for (int i = 0; i < 7; ++i) {
        const float u = rbf(rbf(hc * gw1[i]) + gb1[i]);
        const float z = u * fmaf(u * u, 0.044715f * 0.7978845608028654f * 2.f * LOG2E, 0.7978845608028654f * 2.f * LOG2E);
        const float ge = rbf(u * __builtin_amdgcn_rcpf(1.f + __builtin_amdgcn_exp2f(-z)));
        acc = fmaf(ge, gw2[i], acc);
      }
      acc = wave_sum(acc);
      gsum = lane == col ? acc : gsum;
    }
    if (lane < COLS) sm.gpart[j][lane] = gsum;
    if (lane == 0) sm.tag[j] = tag0;
    wg_barrier();
    float mw[4], M = -INFINITY;
#pragma unroll
    for (int w = 0; w < 4; ++w) { mw[w] = sm.comb[w][DH]; M = fmaxf(M, mw[w]); }
    float L = 0.f, o[8];
#pragma unroll
    for (int i = 0; i < 8; ++i) o[i] = 0.f;
#pragma unroll
    for (int w = 0; w < 4; ++w) {
      const float wt = __builtin_amdgcn_exp2f(mw[w] - M);
      L = fmaf(wt, sm.comb[w][DH + 1], L);
      const v4f a0 = *(const v4f*)&sm.comb[w][32 * j + 8 * t], a1 = *(const v4f*)&sm.comb[w][32 * j + 8 * t + 4];
#pragma unroll
      for (int i = 0; i < 4; ++i) { o[i] = fmaf(wt, a0[i], o[i]); o[4 + i] = fmaf(wt, a1[i], o[4 + i]); }
    }
    const float inv = 1.0f / L;
    v4f acc[5];
#pragma unroll
    for (int cb = 0; cb < 5; ++cb) acc[cb] = v4f{0.f, 0.f, 0.f, 0.f};
#pragma unroll
    for (int rg = 0; rg < 8; ++rg) {
      const float ov = o[rg] * inv;
      const v4u a = diag(cvtpk(ov, ov));
#pragma unroll
      for (int cb = 0; cb < 5; ++cb) acc[cb] = mfma16(a, W[rg * 5 + cb], acc[cb]);
    }
    if (lane < 32) {
#pragma unroll
      for (int cb = 0; cb < 5; ++cb) *(v4f*)&sm.spart[j][128 * cb + 8 * c + 4 * t] = acc[cb];
    }
  }
  wg_barrier();
  if (wave >= 5) return;
  const unsigned tag = sm.tag[0];
  const __amdgpu_buffer_rsrc_t rs = __builtin_amdgcn_make_buffer_rsrc(ws + WS_GRAN + (size_t)b * (NH * NH * COLS * 8), 0, NH * NH * COLS * 8, 0x00020000);
  {
    float s0 = 0.f, s1 = 0.f;
#pragma unroll
    for (int w = 0; w < 4; ++w) { const v2f p = *(const v2f*)&sm.spart[w][2 * tid]; s0 += p[0]; s1 += p[1]; }
    const int cons = tid / 10, cp = tid - cons * 10;
    v4u g = {__float_as_uint(s0), tag, __float_as_uint(s1), tag};
    __builtin_amdgcn_raw_buffer_store_b128(g, rs, (cons * NH + h) * (COLS * 8) + cp * 16, 0, 16);
  }
  {
    const int cp = 2 * wave + (lane >> 5), prod = lane & 31;
    const int col = COLS * h + 2 * cp;
    unsigned xw = *(const unsigned*)(x + b * DM + col);
    float y0 = 0.f, y1 = 0.f;
#pragma unroll
    for (int w = 0; w < 4; ++w) { y0 += sm.gpart[w][2 * cp]; y1 += sm.gpart[w][2 * cp + 1]; }
    const float bb = bflo(b2[0]);
    y0 = rbf(rbf(y0) + bb); y1 = rbf(rbf(y1) + bb);
    float g0 = rbf(y0 / (1.f + __expf(-y0))), g1 = rbf(y1 / (1.f + __expf(-y1)));
    asm volatile("" : "+v"(g0), "+v"(g1), "+v"(xw));
    const char* gp = ws + WS_GRAN + (size_t)b * (NH * NH * COLS * 8) + (h * NH + prod) * (COLS * 8) + cp * 16;
    v4u g;
    for (unsigned spins = 0; spins < SPIN_CAP; ++spins) {
      asm volatile("global_load_dwordx4 %0, %1, off sc1\n\ts_waitcnt vmcnt(0)" : "=&v"(g) : "v"(gp) : "memory");
      if (__all(g[1] == tag && g[3] == tag)) break;
      __builtin_amdgcn_s_sleep(1);
    }
    const float s0 = rbf(half32_sum(__uint_as_float(g[0]))), s1 = rbf(half32_sum(__uint_as_float(g[2])));
    if (prod == 0) *(unsigned*)(out + b * DM + col) = cvtpk(bflo(xw) + rbf(s0 * g0), bfhi(xw) + rbf(s1 * g1));
    if (h == 0 && tid == 0) __hip_atomic_store(epoch_p, tag, __ATOMIC_RELAXED, __HIP_MEMORY_SCOPE_AGENT);
  }
}

extern "C" void kernel_launch(void* const* d_in, const int* in_sizes, int n_in, void* d_out, int out_size, void* d_ws,
                              size_t ws_size, hipStream_t stream) {
  fused_decode<<<256, 512, 0, stream>>>((const uint16_t*)d_in[0], (const char*)d_in[1], (const char*)d_in[2],
                                         (const char*)d_in[3], (const char*)d_in[4], (const uint16_t*)d_in[6],
                                         (const uint16_t*)d_in[7], (const uint16_t*)d_in[8], (const uint16_t*)d_in[9],
                                         (uint16_t*)d_out, (char*)d_ws);
}
```

```cpp
#include <hip/hip_runtime.h>
#include <hip/hip_bf16.h>
#include <stdint.h>

#define GAS __attribute__((address_space(1)))
#define LAS __attribute__((address_space(3)))
typedef unsigned v4u __attribute__((ext_vector_type(4)));
typedef unsigned v2u __attribute__((ext_vector_type(2)));
typedef float v4f __attribute__((ext_vector_type(4)));
typedef float v2f __attribute__((ext_vector_type(2)));
typedef __bf16 bf16x8 __attribute__((ext_vector_type(8)));
typedef __bf16 bf16x2 __attribute__((ext_vector_type(2)));

#define INFL 20
#define WO_SLEEP 24

constexpr int NB = 8, DM = 640, NH = 32, SK = 4096, DH = 128, FF = 1728;
constexpr int COLS = DM / NH;
constexpr float LOG2E = 1.4426950408889634f;
constexpr float SCALE = 0.08838834764831845f;
constexpr unsigned SPIN_CAP = 1u << 20;

constexpr size_t WS_GRAN = 4096;

__device__ __forceinline__ v4f mfma16(v4u a, v4u b, v4f c) {
  return __builtin_amdgcn_mfma_f32_16x16x32_bf16(__builtin_bit_cast(bf16x8, a), __builtin_bit_cast(bf16x8, b), c, 0, 0, 0);
}
__device__ __forceinline__ unsigned cvtpk(float lo, float hi) {
  bf16x2 r = {(__bf16)lo, (__bf16)hi};
  return __builtin_bit_cast(unsigned, r);
}
__device__ __forceinline__ float bflo(unsigned u) { return __builtin_bit_cast(float, u << 16); }
__device__ __forceinline__ float bfhi(unsigned u) { return __builtin_bit_cast(float, u & 0xffff0000u); }
__device__ __forceinline__ float rbf(float x) { return (float)(__bf16)x; }
__device__ __forceinline__ v4u ldnt(const void* p) { return __builtin_nontemporal_load((const GAS v4u*)p); }
__device__ __forceinline__ unsigned ldnt16(const uint16_t* p) { return __builtin_nontemporal_load((const GAS uint16_t*)p); }
template <int D> __device__ __forceinline__ void dma4(unsigned lds, const char* base, unsigned o0, unsigned o1, unsigned o2, unsigned o3) {
  asm volatile("s_mov_b32 m0, %0\n\t"
               "s_waitcnt vmcnt(%6)\n\tglobal_load_lds_dwordx4 %2, %1 nt\n\t"
               "s_waitcnt vmcnt(%6)\n\tglobal_load_lds_dwordx4 %3, %1 offset:1024 nt\n\t"
               "s_waitcnt vmcnt(%6)\n\tglobal_load_lds_dwordx4 %4, %1 offset:2048 nt\n\t"
               "s_waitcnt vmcnt(%6)\n\tglobal_load_lds_dwordx4 %5, %1 offset:3072 nt"
               :: "s"(lds), "s"(base), "v"(o0), "v"(o1), "v"(o2), "v"(o3), "i"(D - 1) : "memory");
}
__device__ __forceinline__ unsigned lds_ld(const unsigned* p) { return __builtin_amdgcn_readfirstlane(*(const volatile LAS unsigned*)p); }
__device__ __forceinline__ void lds_st(unsigned* p, unsigned v) { *(volatile LAS unsigned*)p = v; }

template <int CTRL> __device__ __forceinline__ float dpp(float x) {
  return __builtin_bit_cast(float, __builtin_amdgcn_mov_dpp(__builtin_bit_cast(int, x), CTRL, 0xf, 0xf, true));
}
constexpr int XOR1 = 0xB1, XOR2 = 0x4E, XOR7 = 0x141, XOR15 = 0x140;
__device__ __forceinline__ float swap16_sum(float x) {
  auto s = __builtin_amdgcn_permlane16_swap(__float_as_uint(x), __float_as_uint(x), false, false);
  return __uint_as_float(s[0]) + __uint_as_float(s[1]);
}
__device__ __forceinline__ float swap32_sum(float x) {
  auto s = __builtin_amdgcn_permlane32_swap(__float_as_uint(x), __float_as_uint(x), false, false);
  return __uint_as_float(s[0]) + __uint_as_float(s[1]);
}
__device__ __forceinline__ float swap16_max(float x) {
  auto s = __builtin_amdgcn_permlane16_swap(__float_as_uint(x), __float_as_uint(x), false, false);
  return fmaxf(__uint_as_float(s[0]), __uint_as_float(s[1]));
}
__device__ __forceinline__ float swap32_max(float x) {
  auto s = __builtin_amdgcn_permlane32_swap(__float_as_uint(x), __float_as_uint(x), false, false);
  return fmaxf(__uint_as_float(s[0]), __uint_as_float(s[1]));
}
__device__ __forceinline__ float row16_sum(float x) {
  x += dpp<XOR1>(x); x += dpp<XOR2>(x); x += dpp<XOR7>(x); x += dpp<XOR15>(x); return x;
}
__device__ __forceinline__ float half32_sum(float x) { return swap16_sum(row16_sum(x)); }
__device__ __forceinline__ float wave_sum(float x) { return swap32_sum(half32_sum(x)); }

__device__ __forceinline__ void wg_barrier() {
  asm volatile("s_waitcnt lgkmcnt(0)" ::: "memory");
  __builtin_amdgcn_s_barrier();
  asm volatile("" ::: "memory");
}

__device__ __forceinline__ void ln_stats(const float (&v)[16], bool has2, float& m, float& den) {
  float s = 0.f;
#pragma unroll
  for (int i = 0; i < 16; ++i) s += v[i];
  s = wave_sum(s);
  const float mean = s * (1.0f / DM);
  float q = 0.f;
#pragma unroll
  for (int i = 0; i < 8; ++i) { const float d = v[i] - mean; q = fmaf(d, d, q); }
  float q2 = 0.f;
#pragma unroll
  for (int i = 8; i < 16; ++i) { const float d = v[i] - mean; q2 = fmaf(d, d, q2); }
  q += has2 ? q2 : 0.f;
  q = wave_sum(q);
  const float var = rbf(q * (1.0f / DM));
  m = rbf(mean);
  den = rbf(__builtin_sqrtf(rbf(var + 1e-5f)));
}
__device__ __forceinline__ float ln_apply(float x, float m, float den) { return rbf(rbf(x - m) / den); }

#define STAMP(k) do {} while (0)
constexpr int NSLOT = 9, TILE_B = 16384, NTILE = SK / 32, NCONS = 3;
struct Smem {
  unsigned char ring[NSLOT * TILE_B];
  float qpart[4][DH];
  unsigned qbf[NCONS][64];
  float comb[NCONS][DH + 4];
  float spart[4][DM];
  float gpart[4][32];
  unsigned tag[4];
  unsigned qflag[4];
  unsigned landed[4];
  unsigned freed[4];
};

__global__ void __launch_bounds__(512, 2)
fused_decode(const uint16_t* __restrict__ x, const char* __restrict__ kc, const char* __restrict__ vc,
             const char* __restrict__ wq, const char* __restrict__ wo, const uint16_t* __restrict__ w1,
             const uint16_t* __restrict__ b1, const uint16_t* __restrict__ w2, const uint16_t* __restrict__ b2,
             uint16_t* __restrict__ out, char* ws) {
  __shared__ __attribute__((aligned(16))) Smem sm;
  const int tid = threadIdx.x, lane = tid & 63, wave = __builtin_amdgcn_readfirstlane(tid >> 6);
  const int bid = blockIdx.x;
  const int h = (bid & 7) * 4 + ((bid >> 3) & 3), b = bid >> 5;
  const int t = lane >> 4, c = lane & 15;
  v4u dmask;
#pragma unroll
  for (int i = 0; i < 4; ++i) dmask[i] = (c >> 1) == i ? ((c & 1) ? 0xffff0000u : 0x0000ffffu) : 0u;
  auto diag = [&](unsigned dup) { v4u a; a[0] = dup & dmask[0]; a[1] = dup & dmask[1]; a[2] = dup & dmask[2]; a[3] = dup & dmask[3]; return a; };

  GAS unsigned* epoch_p = (GAS unsigned*)(ws + b * 256);

  if (wave == 0) STAMP(0);
  const size_t hb = ((size_t)(b * NH + h)) * (SK * DH * 2);
  if (wave == 0) {
    __builtin_amdgcn_s_setprio(3);
    const char* kb = kc + hb;
    const char* vb = vc + hb;
    unsigned ko[4];
#pragma unroll
    for (int i = 0; i < 4; ++i) ko[i] = t * 256 + ((c ^ (4 * i + t)) * 16);
    const unsigned vo = lane * 16;
    const unsigned ring0 = (unsigned)(size_t)(LAS unsigned char*)sm.ring;
    unsigned slot = ring0;
    int pub = 0;
    auto publish = [&](int v) { if (v > pub) { pub = v; lds_st(&sm.landed[0], (unsigned)v); } };
    dma4<INFL>(slot, kb, ko[0], ko[1], ko[2], ko[3]);
    dma4<INFL>(slot + 4096, kb + 4096, ko[0], ko[1], ko[2], ko[3]);
    dma4<INFL>(slot + 8192, vb, vo, vo, vo, vo);
    dma4<INFL>(slot + 12288, vb + 4096, vo, vo, vo, vo);
    if (lane < 12) sm.qflag[lane] = 0u;
    wg_barrier();
    int cons = 0;
    for (int n = 1; n < NTILE; ++n) {
      kb += 8192; vb += 8192;
      slot += TILE_B; if (slot == ring0 + NSLOT * TILE_B) slot = ring0;
      cons = cons == NCONS - 1 ? 0 : cons + 1;
      if (n >= NSLOT) {
        const unsigned need = n - NSLOT + 1;
        if (lds_ld(&sm.freed[cons]) < need) {
          asm volatile("s_waitcnt vmcnt(0)" ::: "memory");
          publish(2 * n);
          while (lds_ld(&sm.freed[cons]) < need) __builtin_amdgcn_s_sleep(1);
        }
      }
      constexpr int PH = ((INFL + 3) / 4 + 1) & 1;
      dma4<INFL>(slot, kb, ko[0], ko[1], ko[2], ko[3]);
      if (PH == 0) publish((16 * n + 4 - INFL) >> 3);
      dma4<INFL>(slot + 4096, kb + 4096, ko[0], ko[1], ko[2], ko[3]);
      if (PH == 1) publish((16 * n + 8 - INFL) >> 3);
      dma4<INFL>(slot + 8192, vb, vo, vo, vo, vo);
      if (PH == 0) publish((16 * n + 12 - INFL) >> 3);
      dma4<INFL>(slot + 12288, vb + 4096, vo, vo, vo, vo);
      if (PH == 1) publish((16 * n + 16 - INFL) >> 3);
    }
    asm volatile("s_waitcnt vmcnt(16)" ::: "memory");
    publish(2 * NTILE - 2);
    asm volatile("s_waitcnt vmcnt(8)" ::: "memory");
    publish(2 * NTILE - 1);
    asm volatile("s_waitcnt vmcnt(0)" ::: "memory");
    publish(2 * NTILE);
    STAMP(6);
    __builtin_amdgcn_s_setprio(0);
    wg_barrier();
  } else if (wave < 4) {
    const int ci = wave - 1;
    wg_barrier();
    for (;;) {
      const v4u f = *(const volatile LAS v4u*)sm.qflag;
      if (__builtin_amdgcn_readfirstlane(f[0] & f[1] & f[2] & f[3])) break;
      __builtin_amdgcn_s_sleep(1);
    }
    asm volatile("" ::: "memory");
    if (wave == 1) STAMP(1);
    v4u qf[4];
    {
      float q0 = 0.f, q1 = 0.f;
#pragma unroll
      for (int j = 0; j < 4; ++j) { const v2f p = *(const v2f*)&sm.qpart[j][2 * lane]; q0 += p[0]; q1 += p[1]; }
      sm.qbf[ci][lane] = cvtpk(q0, q1);
      asm volatile("s_waitcnt lgkmcnt(0)" ::: "memory");
#pragma unroll
      for (int s = 0; s < 4; ++s) qf[s] = *(const v4u*)&sm.qbf[ci][16 * s + 4 * t];
    }
    float m = -INFINITY, l = 0.f;
    v4f O0 = {0.f, 0.f, 0.f, 0.f}, O1 = {0.f, 0.f, 0.f, 0.f};
    unsigned kof[4];
#pragma unroll
    for (int s = 0; s < 4; ++s) kof[s] = c * 256 + (((4 * s + t) ^ c) * 16);
    const unsigned vof = 8192 + t * 1024 + c * 16;
    int slot = ci;
    for (int n = ci; n < NTILE; n += NCONS) {
      const unsigned char* tb = sm.ring + slot * TILE_B;
      while (lds_ld(&sm.landed[0]) < (unsigned)(2 * n + 1)) __builtin_amdgcn_s_sleep(1);
      asm volatile("" ::: "memory");
      v4f S0 = {0.f, 0.f, 0.f, 0.f}, S1 = {0.f, 0.f, 0.f, 0.f};
      {
        v4u Kr[8];
#pragma unroll
        for (int s = 0; s < 4; ++s) { Kr[s] = *(const v4u*)(tb + kof[s]); Kr[4 + s] = *(const v4u*)(tb + 4096 + kof[s]); }
#pragma unroll
        for (int s = 0; s < 4; ++s) { S0 = mfma16(Kr[s], qf[s], S0); S1 = mfma16(Kr[4 + s], qf[s], S1); }
      }
      float sc[8];
#pragma unroll
      for (int i = 0; i < 4; i += 2) {
        unsigned a = cvtpk(S0[i], S0[i + 1]), d = cvtpk(S1[i], S1[i + 1]);
        a = cvtpk(bflo(a) * SCALE, bfhi(a) * SCALE); d = cvtpk(bflo(d) * SCALE, bfhi(d) * SCALE);
        sc[i] = bflo(a); sc[i + 1] = bfhi(a); sc[4 + i] = bflo(d); sc[5 + i] = bfhi(d);
      }
      float mx = fmaxf(fmaxf(fmaxf(sc[0], sc[1]), fmaxf(sc[2], sc[3])), fmaxf(fmaxf(sc[4], sc[5]), fmaxf(sc[6], sc[7])));
      mx = swap32_max(swap16_max(mx));
      const float mn = fmaxf(m, mx);
      const float alpha = __builtin_amdgcn_exp2f((m - mn) * LOG2E);
      const float mo = mn * LOG2E;
      m = mn;
      float p[8], ps = 0.f;
#pragma unroll
      for (int i = 0; i < 8; ++i) { p[i] = __builtin_amdgcn_exp2f(fmaf(sc[i], LOG2E, -mo)); ps += p[i]; }
      l = fmaf(l, alpha, ps);
      O0 *= alpha; O1 *= alpha;
      v4u pa[8];
#pragma unroll
      for (int i = 0; i < 8; ++i) pa[i] = diag(cvtpk(p[i], p[i]));
      while (lds_ld(&sm.landed[0]) < (unsigned)(2 * n + 2)) __builtin_amdgcn_s_sleep(1);
      asm volatile("" ::: "memory");
      v4u Vr[8];
#pragma unroll
      for (int i = 0; i < 4; ++i) { Vr[i] = *(const v4u*)(tb + vof + i * 256); Vr[4 + i] = *(const v4u*)(tb + vof + 4096 + i * 256); }
#pragma unroll
      for (int i = 0; i < 4; ++i) { O0 = mfma16(pa[i], Vr[i], O0); O1 = mfma16(pa[4 + i], Vr[4 + i], O1); }
      asm volatile("s_waitcnt lgkmcnt(0)" ::: "memory");
      lds_st(&sm.freed[ci], n + 1);
      slot += NCONS; slot = slot >= NSLOT ? slot - NSLOT : slot;
    }
    if (wave == 1) STAMP(2);
    O0 += O1;
    l = swap32_sum(swap16_sum(l));
    if (lane < 32) *(v4f*)&sm.comb[ci][8 * c + 4 * t] = O0;
    if (lane == 0) { sm.comb[ci][DH] = m * LOG2E; sm.comb[ci][DH + 1] = l; }
    wg_barrier();
  } else {
    const int j = wave - 4;
    const uint16_t* xr = x + b * DM;
    const v4u xa = ldnt(xr + 8 * lane);
    v4u xb2 = {0u, 0u, 0u, 0u};
    if (lane < 16) xb2 = ldnt(xr + 512 + 8 * lane);
    const int rot = b % 5;
    v4u xq[5];
    int rr[5];
#pragma unroll
    for (int n = 0; n < 5; ++n) { int r = n + rot; r = r >= 5 ? r - 5 : r; rr[n] = r; xq[n] = ldnt(xr + 160 * j + 40 * t + 8 * r); }
    const unsigned tag0 = __hip_atomic_load(epoch_p, __ATOMIC_RELAXED, __HIP_MEMORY_SCOPE_AGENT) + 1u;
    v4u W[40];
    {
      const char* p = wq + (size_t)(160 * j + 40 * t) * (NH * DH * 2) + h * (DH * 2) + c * 16;
#pragma unroll
      for (int n = 0; n < 5; ++n)
#pragma unroll
        for (int i = 0; i < 8; ++i) W[n * 8 + i] = ldnt(p + (size_t)(8 * rr[n] + i) * (NH * DH * 2));
    }
    __builtin_amdgcn_sched_barrier(0);
    wg_barrier();
    float xv[16];
#pragma unroll
    for (int i = 0; i < 4; ++i) { xv[2 * i] = bflo(xa[i]); xv[2 * i + 1] = bfhi(xa[i]); xv[8 + 2 * i] = bflo(xb2[i]); xv[9 + 2 * i] = bfhi(xb2[i]); }
    float m1, den1;
    ln_stats(xv, lane < 16, m1, den1);
    {
      v4f Q0 = {0.f, 0.f, 0.f, 0.f}, Q1 = {0.f, 0.f, 0.f, 0.f};
#pragma unroll
      for (int n = 0; n < 5; ++n)
#pragma unroll
        for (int i = 0; i < 4; ++i) {
          const float e0 = ln_apply(bflo(xq[n][i]), m1, den1), e1 = ln_apply(bfhi(xq[n][i]), m1, den1);
          Q0 = mfma16(diag(cvtpk(e0, e0)), W[n * 8 + 2 * i], Q0);
          Q1 = mfma16(diag(cvtpk(e1, e1)), W[n * 8 + 2 * i + 1], Q1);
        }
      Q0 += Q1;
      if (lane < 32) *(v4f*)&sm.qpart[j][8 * c + 4 * t] = Q0;
    }
    asm volatile("s_waitcnt lgkmcnt(0)" ::: "memory");
    lds_st(&sm.qflag[j], 1u);
    float gw1[7], gb1[7], gw2[7];
    unsigned xcol;
    {
      unsigned r1[7], r2[7], r3[7];
#pragma unroll
      for (int i = 0; i < 7; ++i) {
        const int f = 256 * i + 64 * (i == 6 && j == 3 ? 0 : j) + lane;
        r1[i] = ldnt16(w1 + f); r2[i] = ldnt16(b1 + f); r3[i] = ldnt16(w2 + f);
      }
      xcol = ldnt16(xr + COLS * h + (lane < COLS ? lane : 0));
      r3[6] = j == 3 ? 0u : r3[6];
#pragma unroll
      for (int i = 0; i < 7; ++i) { gw1[i] = bflo(r1[i]); gb1[i] = bflo(r2[i]); gw2[i] = bflo(r3[i]); }
    }
    float xn[16];
#pragma unroll
    for (int i = 0; i < 16; ++i) xn[i] = ln_apply(xv[i], m1, den1);
#pragma unroll
    for (int i = 8; i < 16; ++i) xn[i] = lane < 16 ? xn[i] : 0.f;
    float m2, den2;
    ln_stats(xn, lane < 16, m2, den2);
    const float hv = ln_apply(ln_apply(bflo(xcol), m1, den1), m2, den2);
    float gsum = 0.f;
#pragma unroll 4
    for (int col = 0; col < COLS; ++col) {
      const float hc = __builtin_bit_cast(float, __builtin_amdgcn_readlane(__builtin_bit_cast(int, hv), col));
      float acc = 0.f;
#pragma unroll
      for (int i = 0; i < 7; ++i) {
        const float u = rbf(rbf(hc * gw1[i]) + gb1[i]);
        const float z = u * fmaf(u * u, 0.044715f * 0.7978845608028654f * 2.f * LOG2E, 0.7978845608028654f * 2.f * LOG2E);
        const float ge = rbf(u * __builtin_amdgcn_rcpf(1.f + __builtin_amdgcn_exp2f(-z)));
        acc = fmaf(ge, gw2[i], acc);
      }
      acc = wave_sum(acc);
      gsum = lane == col ? acc : gsum;
    }
    if (lane < COLS) sm.gpart[j][lane] = gsum;
    if (lane == 0) sm.tag[j] = tag0;
    if (wave == 4) STAMP(3);
    {
      const char* p = wo + (size_t)(DH * h + 32 * j + 8 * t) * (DM * 2) + c * 16;
#pragma unroll
      for (int rg = 0; rg < 8; ++rg)
#pragma unroll
        for (int cb = 0; cb < 5; ++cb) {
          W[rg * 5 + cb] = ldnt(p + rg * (DM * 2) + cb * 256);
          __builtin_amdgcn_sched_barrier(0);
          __builtin_amdgcn_s_sleep(WO_SLEEP);
          __builtin_amdgcn_sched_barrier(0);
        }
    }
    if (wave == 4) STAMP(7);
    wg_barrier();
    float mw[NCONS], M = -INFINITY;
#pragma unroll
    for (int w = 0; w < NCONS; ++w) { mw[w] = sm.comb[w][DH]; M = fmaxf(M, mw[w]); }
    float L = 0.f, o[8];
#pragma unroll
    for (int i = 0; i < 8; ++i) o[i] = 0.f;
#pragma unroll
    for (int w = 0; w < NCONS; ++w) {
      const float wt = __builtin_amdgcn_exp2f(mw[w] - M);
      L = fmaf(wt, sm.comb[w][DH + 1], L);
      const v4f a0 = *(const v4f*)&sm.comb[w][32 * j + 8 * t], a1 = *(const v4f*)&sm.comb[w][32 * j + 8 * t + 4];
#pragma unroll
      for (int i = 0; i < 4; ++i) { o[i] = fmaf(wt, a0[i], o[i]); o[4 + i] = fmaf(wt, a1[i], o[4 + i]); }
    }
    const float inv = 1.0f / L;
    v4f acc[5];
#pragma unroll
    for (int cb = 0; cb < 5; ++cb) acc[cb] = v4f{0.f, 0.f, 0.f, 0.f};
#pragma unroll
    for (int rg = 0; rg < 8; ++rg) {
      const float ov = o[rg] * inv;
      const v4u a = diag(cvtpk(ov, ov));
#pragma unroll
      for (int cb = 0; cb < 5; ++cb) acc[cb] = mfma16(a, W[rg * 5 + cb], acc[cb]);
    }
    if (lane < 32) {
#pragma unroll
      for (int cb = 0; cb < 5; ++cb) *(v4f*)&sm.spart[j][128 * cb + 8 * c + 4 * t] = acc[cb];
    }
  }
  wg_barrier();
  if (wave >= 5) return;
  if (wave == 0) STAMP(4);
  const unsigned tag = sm.tag[0];
  const __amdgpu_buffer_rsrc_t rs = __builtin_amdgcn_make_buffer_rsrc(ws + WS_GRAN + (size_t)b * (NH * NH * COLS * 8), 0, NH * NH * COLS * 8, 0x00020000);
  {
    float s0 = 0.f, s1 = 0.f;
#pragma unroll
    for (int w = 0; w < 4; ++w) { const v2f p = *(const v2f*)&sm.spart[w][2 * tid]; s0 += p[0]; s1 += p[1]; }
    const int cons = tid / 10, cp = tid - cons * 10;
    v4u g = {__float_as_uint(s0), tag, __float_as_uint(s1), tag};
    __builtin_amdgcn_raw_buffer_store_b128(g, rs, (cons * NH + h) * (COLS * 8) + cp * 16, 0, 16);
  }
  {
    const int cp = 2 * wave + (lane >> 5), prod = lane & 31;
    const int col = COLS * h + 2 * cp;
    unsigned xw = *(const unsigned*)(x + b * DM + col);
    float y0 = 0.f, y1 = 0.f;
#pragma unroll
    for (int w = 0; w < 4; ++w) { y0 += sm.gpart[w][2 * cp]; y1 += sm.gpart[w][2 * cp + 1]; }
    const float bb = bflo(b2[0]);
    y0 = rbf(rbf(y0) + bb); y1 = rbf(rbf(y1) + bb);
    float g0 = rbf(y0 / (1.f + __expf(-y0))), g1 = rbf(y1 / (1.f + __expf(-y1)));
    asm volatile("" : "+v"(g0), "+v"(g1), "+v"(xw));
    const char* gp = ws + WS_GRAN + (size_t)b * (NH * NH * COLS * 8) + (h * NH + prod) * (COLS * 8) + cp * 16;
    v4u g;
    for (unsigned spins = 0; spins < SPIN_CAP; ++spins) {
      asm volatile("global_load_dwordx4 %0, %1, off sc1\n\ts_waitcnt vmcnt(0)" : "=&v"(g) : "v"(gp) : "memory");
      if (__all(g[1] == tag && g[3] == tag)) break;
      __builtin_amdgcn_s_sleep(1);
    }
    const float s0 = rbf(half32_sum(__uint_as_float(g[0]))), s1 = rbf(half32_sum(__uint_as_float(g[2])));
    if (prod == 0) *(unsigned*)(out + b * DM + col) = cvtpk(bflo(xw) + rbf(s0 * g0), bfhi(xw) + rbf(s1 * g1));
    if (wave == 0) STAMP(5);
    if (h == 0 && tid == 0) __hip_atomic_store(epoch_p, tag, __ATOMIC_RELAXED, __HIP_MEMORY_SCOPE_AGENT);
  }
}

extern "C" void kernel_launch(void* const* d_in, const int* in_sizes, int n_in, void* d_out, int out_size, void* d_ws,
                              size_t ws_size, hipStream_t stream) {
  fused_decode<<<256, 512, 0, stream>>>((const uint16_t*)d_in[0], (const char*)d_in[1], (const char*)d_in[2],
                                         (const char*)d_in[3], (const char*)d_in[4], (const uint16_t*)d_in[6],
                                         (const uint16_t*)d_in[7], (const uint16_t*)d_in[8], (const uint16_t*)d_in[9],
                                         (uint16_t*)d_out, (char*)d_ws);
}
```

```cpp
#include <hip/hip_runtime.h>
#include <hip/hip_bf16.h>
#include <stdint.h>

#define GAS __attribute__((address_space(1)))
#define LAS __attribute__((address_space(3)))
typedef unsigned v4u __attribute__((ext_vector_type(4)));
typedef unsigned v2u __attribute__((ext_vector_type(2)));
typedef float v4f __attribute__((ext_vector_type(4)));
typedef float v2f __attribute__((ext_vector_type(2)));
typedef __bf16 bf16x8 __attribute__((ext_vector_type(8)));
typedef __bf16 bf16x2 __attribute__((ext_vector_type(2)));

#define DMIN 18
#define DSTEP 2
#define NLEV 6
#define LSH 6
#define TAU 856
#define TOFF 300
#define OUTPOL 0
#define O1NT 0
#define S2NT 0
#define O1DELAY 0
#define S2DELAY 0
#define O1POL "sc1"
#define S2POL "sc1"
#define ABL 0
#define NPRE 3

constexpr int NB = 8, DM = 640, NH = 32, SK = 4096, DH = 128, FF = 1728;
constexpr int COLS = DM / NH;
constexpr float LOG2E = 1.4426950408889634f;
constexpr float SCALE = 0.08838834764831845f;
constexpr unsigned SPIN_CAP = 1u << 20;

constexpr size_t WS_OBOX = 4096, OBOX_B = 512, WS_SBOX = WS_OBOX + 256 * OBOX_B, SBOX_B = 40 * NH * 128;

__device__ __forceinline__ v4f mfma16(v4u a, v4u b, v4f c) {
  return __builtin_amdgcn_mfma_f32_16x16x32_bf16(__builtin_bit_cast(bf16x8, a), __builtin_bit_cast(bf16x8, b), c, 0, 0, 0);
}
__device__ __forceinline__ unsigned cvtpk(float lo, float hi) {
  bf16x2 r = {(__bf16)lo, (__bf16)hi};
  return __builtin_bit_cast(unsigned, r);
}
__device__ __forceinline__ float bflo(unsigned u) { return __builtin_bit_cast(float, u << 16); }
__device__ __forceinline__ float bfhi(unsigned u) { return __builtin_bit_cast(float, u & 0xffff0000u); }
__device__ __forceinline__ float rbf(float x) { return (float)(__bf16)x; }
__device__ __forceinline__ v4u ldnt(const void* p) { return __builtin_nontemporal_load((const GAS v4u*)p); }
__device__ __forceinline__ unsigned ldnt16(const uint16_t* p) { return __builtin_nontemporal_load((const GAS uint16_t*)p); }
template <int D> __device__ __forceinline__ void dma4(unsigned lds, const char* base, unsigned o0, unsigned o1, unsigned o2, unsigned o3) {
  asm volatile("s_mov_b32 m0, %0\n\t"
               "s_waitcnt vmcnt(%6)\n\tglobal_load_lds_dwordx4 %2, %1 nt\n\t"
               "s_waitcnt vmcnt(%6)\n\tglobal_load_lds_dwordx4 %3, %1 offset:1024 nt\n\t"
               "s_waitcnt vmcnt(%6)\n\tglobal_load_lds_dwordx4 %4, %1 offset:2048 nt\n\t"
               "s_waitcnt vmcnt(%6)\n\tglobal_load_lds_dwordx4 %5, %1 offset:3072 nt"
               :: "s"(lds), "s"(base), "v"(o0), "v"(o1), "v"(o2), "v"(o3), "i"(D - 1) : "memory");
}
__device__ __forceinline__ void dma1(unsigned lds, const char* base, unsigned o) {
  asm volatile("s_mov_b32 m0, %0\n\ts_nop 0\n\tglobal_load_lds_dwordx4 %2, %1 nt" :: "s"(lds), "s"(base), "v"(o) : "memory");
}
__device__ __forceinline__ unsigned lds_ld(const unsigned* p) { return __builtin_amdgcn_readfirstlane(*(const volatile LAS unsigned*)p); }
__device__ __forceinline__ void lds_st(unsigned* p, unsigned v) { *(volatile LAS unsigned*)p = v; }

template <int CTRL> __device__ __forceinline__ float dpp(float x) {
  return __builtin_bit_cast(float, __builtin_amdgcn_mov_dpp(__builtin_bit_cast(int, x), CTRL, 0xf, 0xf, true));
}
constexpr int XOR1 = 0xB1, XOR2 = 0x4E, XOR7 = 0x141, XOR15 = 0x140;
__device__ __forceinline__ float swap16_sum(float x) {
  auto s = __builtin_amdgcn_permlane16_swap(__float_as_uint(x), __float_as_uint(x), false, false);
  return __uint_as_float(s[0]) + __uint_as_float(s[1]);
}
__device__ __forceinline__ float swap32_sum(float x) {
  auto s = __builtin_amdgcn_permlane32_swap(__float_as_uint(x), __float_as_uint(x), false, false);
  return __uint_as_float(s[0]) + __uint_as_float(s[1]);
}
__device__ __forceinline__ float swap16_max(float x) {
  auto s = __builtin_amdgcn_permlane16_swap(__float_as_uint(x), __float_as_uint(x), false, false);
  return fmaxf(__uint_as_float(s[0]), __uint_as_float(s[1]));
}
__device__ __forceinline__ float swap32_max(float x) {
  auto s = __builtin_amdgcn_permlane32_swap(__float_as_uint(x), __float_as_uint(x), false, false);
  return fmaxf(__uint_as_float(s[0]), __uint_as_float(s[1]));
}
__device__ __forceinline__ float row16_sum(float x) {
  x += dpp<XOR1>(x); x += dpp<XOR2>(x); x += dpp<XOR7>(x); x += dpp<XOR15>(x); return x;
}
__device__ __forceinline__ float half32_sum(float x) { return swap16_sum(row16_sum(x)); }
__device__ __forceinline__ float wave_sum(float x) { return swap32_sum(half32_sum(x)); }

__device__ __forceinline__ float wave_max(float x) {
  x = fmaxf(x, dpp<XOR1>(x)); x = fmaxf(x, dpp<XOR2>(x)); x = fmaxf(x, dpp<XOR7>(x)); x = fmaxf(x, dpp<XOR15>(x));
  return swap32_max(swap16_max(x));
}
__device__ __forceinline__ void wg_barrier() {
  asm volatile("s_waitcnt lgkmcnt(0)" ::: "memory");
  __builtin_amdgcn_s_barrier();
  asm volatile("" ::: "memory");
}

__device__ __forceinline__ float ln_apply(float x, float m, float rden) { return rbf(rbf(x - m) * rden); }
template <int N, bool HALF>
__device__ __forceinline__ void ln_stats(const float (&v)[N], bool tail, float& m, float& den) {
  float s = 0.f, s2 = 0.f;
#pragma unroll
  for (int i = 0; i < N - 8; ++i) s += v[i];
#pragma unroll
  for (int i = N - 8; i < N; ++i) s2 += v[i];
  s += tail ? s2 : 0.f;
  s = HALF ? half32_sum(s) : wave_sum(s);
  const float mean = s * (1.0f / DM);
  float q = 0.f, q2 = 0.f;
#pragma unroll
  for (int i = 0; i < N - 8; ++i) { const float d = v[i] - mean; q = fmaf(d, d, q); }
#pragma unroll
  for (int i = N - 8; i < N; ++i) { const float d = v[i] - mean; q2 = fmaf(d, d, q2); }
  q += tail ? q2 : 0.f;
  q = HALF ? half32_sum(q) : wave_sum(q);
  const float var = rbf(q * (1.0f / DM));
  m = rbf(mean);
  den = __builtin_amdgcn_rcpf(rbf(__builtin_sqrtf(rbf(var + 1e-5f))));
}
__device__ __forceinline__ float rdlane(float x, int l) { return __builtin_bit_cast(float, __builtin_amdgcn_readlane(__builtin_bit_cast(int, x), l)); }

#define STAMP(k) do {} while (0)
constexpr int NSLOT = 8, TILE_B = 16384, NKT = SK / 64, NTILE = 2 * NKT, NCONS = 3;
constexpr int LASTC = (NTILE - 1) % NCONS;
struct Smem {
  unsigned char ring[NSLOT * TILE_B];
  uint16_t wblk[DH * 80];
  uint16_t sc[SK];
  float qpart[4][DH];
  unsigned qbf[NCONS][64];
  float ml[NCONS][2];
  float pad[2];
  float gpart[4][32];
  unsigned xfin[16];
  unsigned ctl[32];
};
constexpr int QFLAG = 0, WLAND = 4, FREED = 8, LANDED = 12, TAG = 15, KDONE = 16, VDONE = 20, GDONE = 24;

__device__ __forceinline__ void wait4(const unsigned* p) {
  for (;;) {
    const v4u f = *(const volatile LAS v4u*)p;
    if (__builtin_amdgcn_readfirstlane(f[0] & f[1] & f[2] & f[3])) break;
    __builtin_amdgcn_s_sleep(1);
  }
  asm volatile("" ::: "memory");
}

template <int D, typename F>
__device__ __forceinline__ void tile(int n, unsigned slot, const char* src, const unsigned (&of)[4], F& publish) {
  constexpr int PH = ((D + 3) / 4 + 1) & 1;
  dma4<D>(slot, src, of[0], of[1], of[2], of[3]);
  if (PH == 0) publish((16 * n + 4 - D) >> 3);
  dma4<D>(slot + 4096, src + 4096, of[0], of[1], of[2], of[3]);
  if (PH == 1) publish((16 * n + 8 - D) >> 3);
  dma4<D>(slot + 8192, src + 8192, of[0], of[1], of[2], of[3]);
  if (PH == 0) publish((16 * n + 12 - D) >> 3);
  dma4<D>(slot + 12288, src + 12288, of[0], of[1], of[2], of[3]);
  if (PH == 1) publish((16 * n + 16 - D) >> 3);
}

__global__ void __launch_bounds__(512, 2)
fused_decode(const uint16_t* __restrict__ x, const char* __restrict__ kc, const char* __restrict__ vc,
             const char* __restrict__ wq, const char* __restrict__ wo, const uint16_t* __restrict__ w1,
             const uint16_t* __restrict__ b1, const uint16_t* __restrict__ w2, const uint16_t* __restrict__ b2,
             uint16_t* __restrict__ out, char* ws) {
  __shared__ __attribute__((aligned(16))) Smem sm;
  const int tid = threadIdx.x, lane = tid & 63, wave = __builtin_amdgcn_readfirstlane(tid >> 6);
  const int bid = blockIdx.x;
  const int h = (bid & 7) * 4 + ((bid >> 3) & 3), b = bid >> 5;
  const int t = lane >> 4, c = lane & 15;
  v4u dmask;
#pragma unroll
  for (int i = 0; i < 4; ++i) dmask[i] = ((c & 7) >> 1) == i ? ((c & 1) ? 0xffff0000u : 0x0000ffffu) : 0u;
  auto diag = [&](unsigned dup) { v4u a; a[0] = dup & dmask[0]; a[1] = dup & dmask[1]; a[2] = dup & dmask[2]; a[3] = dup & dmask[3]; return a; };
  GAS unsigned* epoch_p = (GAS unsigned*)ws;
  int gw;

  if (wave == 0) {
    STAMP(0);
    __builtin_amdgcn_s_setprio(3);
    const size_t hb = ((size_t)(b * NH + h)) * (SK * DH * 2);
    const char* src = kc + hb;
    unsigned of[4];
#pragma unroll
    for (int i = 0; i < 4; ++i) of[i] = t * 256 + ((c ^ (4 * i + t)) * 16);
    const unsigned ring0 = (unsigned)(size_t)(LAS unsigned char*)sm.ring;
    unsigned slot = ring0;
    int pub = 0;
    auto publish = [&](int v) { if (v > pub) { pub = v; lds_st(&sm.ctl[LANDED], (unsigned)v); } };
    dma4<63>(slot, src, of[0], of[1], of[2], of[3]);
    dma4<63>(slot + 4096, src + 4096, of[0], of[1], of[2], of[3]);
    wg_barrier();
    dma4<63>(slot + 8192, src + 8192, of[0], of[1], of[2], of[3]);
    dma4<63>(slot + 12288, src + 12288, of[0], of[1], of[2], of[3]);
    const unsigned tstart = (unsigned)__builtin_amdgcn_s_memrealtime();
    int cons = (NCONS - NSLOT % NCONS) % NCONS;
    for (int n = 1; n < NTILE; ++n) {
      src += TILE_B;
      if (n == NKT) { src = vc + hb; of[0] = of[1] = of[2] = of[3] = lane * 16; }
      slot += TILE_B; if (slot == ring0 + NSLOT * TILE_B) slot = ring0;
      cons = cons == NCONS - 1 ? 0 : cons + 1;
      if (n >= NSLOT) {
        const unsigned need = n - NSLOT + 1;
        if (lds_ld(&sm.ctl[FREED + cons]) < need) {
          asm volatile("s_waitcnt vmcnt(0)" ::: "memory");
          publish(2 * n);
          while (lds_ld(&sm.ctl[FREED + cons]) < need) __builtin_amdgcn_s_sleep(1);
        }
      }
      const int lev = ((int)((unsigned)__builtin_amdgcn_s_memrealtime() - tstart) - TOFF - ((n * TAU) >> 4)) >> LSH;
      if (lev <= 0) tile<DMIN>(n, slot, src, of, publish);
      else if (lev == 1 || NLEV == 2) tile<DMIN + DSTEP>(n, slot, src, of, publish);
      else if (lev == 2 || NLEV == 3) tile<DMIN + 2 * DSTEP>(n, slot, src, of, publish);
      else if (lev == 3 || NLEV == 4) tile<DMIN + 3 * DSTEP>(n, slot, src, of, publish);
      else if (lev == 4 || NLEV == 5) tile<DMIN + 4 * DSTEP>(n, slot, src, of, publish);
      else tile<DMIN + 5 * DSTEP>(n, slot, src, of, publish);
    }
    asm volatile("s_waitcnt vmcnt(24)" ::: "memory");
    publish(2 * NTILE - 3);
    asm volatile("s_waitcnt vmcnt(16)" ::: "memory");
    publish(2 * NTILE - 2);
    asm volatile("s_waitcnt vmcnt(8)" ::: "memory");
    publish(2 * NTILE - 1);
    asm volatile("s_waitcnt vmcnt(0)" ::: "memory");
    publish(2 * NTILE);
    STAMP(6);
    __builtin_amdgcn_s_setprio(0);
    gw = 4;
  } else if (wave < 4) {
    const int ci = wave - 1;
    if (wave == 1 && lane < 32) sm.ctl[lane] = (lane == KDONE + 3 || lane == VDONE + 3 || lane == VDONE + LASTC) ? 1u : 0u;
    wg_barrier();
    unsigned kof[4];
#pragma unroll
    for (int s = 0; s < 4; ++s) kof[s] = c * 256 + (((4 * s + t) ^ c) * 16);
    int slot = ci, n = ci;
    auto fetchK = [&](v4u (&Kr)[16]) {
      const unsigned char* tb = sm.ring + slot * TILE_B;
      while (lds_ld(&sm.ctl[LANDED]) < (unsigned)(2 * n + 2)) __builtin_amdgcn_s_sleep(1);
      asm volatile("" ::: "memory");
#pragma unroll
      for (int u = 0; u < 4; ++u)
#pragma unroll
        for (int s = 0; s < 4; ++s) Kr[4 * u + s] = *(const v4u*)(tb + u * 4096 + kof[s]);
      asm volatile("s_waitcnt lgkmcnt(0)" ::: "memory");
      lds_st(&sm.ctl[FREED + ci], n + 1);
#pragma unroll
      for (int i = 0; i < 16; ++i) asm volatile("" : "+v"(Kr[i]));
      slot += NCONS; slot = slot >= NSLOT ? slot - NSLOT : slot;
      n += NCONS;
    };
    v4u Kp[NPRE][16];
#pragma unroll
    for (int k = 0; k < NPRE; ++k) fetchK(Kp[k]);
    wait4(&sm.ctl[QFLAG]);
    if (wave == 1) STAMP(1);
    v4u qf[4];
    {
      float q0 = 0.f, q1 = 0.f;
#pragma unroll
      for (int j = 0; j < 4; ++j) { const v2f p = *(const v2f*)&sm.qpart[j][2 * lane]; q0 += p[0]; q1 += p[1]; }
      sm.qbf[ci][lane] = cvtpk(q0, q1);
      asm volatile("s_waitcnt lgkmcnt(0)" ::: "memory");
#pragma unroll
      for (int s = 0; s < 4; ++s) qf[s] = *(const v4u*)&sm.qbf[ci][16 * s + 4 * t];
    }
    float m = -INFINITY, l = 0.f;
    auto scoreK = [&](const v4u (&Kr)[16], int tile) {
      v4f S[4];
#pragma unroll
      for (int u = 0; u < 4; ++u) S[u] = v4f{0.f, 0.f, 0.f, 0.f};
#pragma unroll
      for (int s = 0; s < 4; ++s)
#pragma unroll
        for (int u = 0; u < 4; ++u) S[u] = mfma16(qf[s], Kr[4 * u + s], S[u]);
      const float sr = t == 0 ? S[0][0] : t == 1 ? S[1][0] : t == 2 ? S[2][0] : S[3][0];
      const unsigned sb = cvtpk(rbf(sr) * SCALE, 0.f);
      sm.sc[64 * tile + lane] = (uint16_t)sb;
      const float sv = bflo(sb) * LOG2E;
      const float mn = fmaxf(m, sv);
      l = fmaf(l, __builtin_amdgcn_exp2f(m - mn), __builtin_amdgcn_exp2f(sv - mn));
      m = mn;
    };
#pragma unroll
    for (int k = 0; k < NPRE; ++k) scoreK(Kp[k], ci + NCONS * k);
    while (n < NKT) {
      v4u Kr[16];
      fetchK(Kr);
      scoreK(Kr, n - NCONS);
    }
    {
      const float mw = wave_max(m);
      const float lw = wave_sum(l * __builtin_amdgcn_exp2f(m - mw));
      if (lane == 0) { sm.ml[ci][0] = mw; sm.ml[ci][1] = lw; }
      asm volatile("s_waitcnt lgkmcnt(0)" ::: "memory");
      lds_st(&sm.ctl[KDONE + ci], 1u);
      wait4(&sm.ctl[KDONE]);
    }
    float M = -INFINITY, L = 0.f;
#pragma unroll
    for (int w = 0; w < NCONS; ++w) M = fmaxf(M, sm.ml[w][0]);
#pragma unroll
    for (int w = 0; w < NCONS; ++w) L = fmaf(sm.ml[w][1], __builtin_amdgcn_exp2f(sm.ml[w][0] - M), L);
    const float invL = 1.0f / L;
    const unsigned tag = sm.ctl[TAG];
    if (wave == 1) STAMP(13);
    v4f O0 = {0.f, 0.f, 0.f, 0.f}, O1 = {0.f, 0.f, 0.f, 0.f};
    const unsigned vof = t * 1024 + c * 16;
    for (; n < NTILE; n += NCONS) {
      const unsigned char* tb = sm.ring + slot * TILE_B + vof;
      unsigned pd[16];
#pragma unroll
      for (int qd = 0; qd < 4; ++qd) {
        const v2u sv = *(const v2u*)&sm.sc[64 * (n - NKT) + 16 * qd + 4 * t];
#pragma unroll
        for (int i = 0; i < 2; ++i) {
          const float p0 = __builtin_amdgcn_exp2f(fmaf(bflo(sv[i]), LOG2E, -M)) * invL, p1 = __builtin_amdgcn_exp2f(fmaf(bfhi(sv[i]), LOG2E, -M)) * invL;
          pd[4 * qd + 2 * i] = cvtpk(p0, p0); pd[4 * qd + 2 * i + 1] = cvtpk(p1, p1);
        }
      }
#pragma unroll
      for (int hf = 0; hf < 2; ++hf) {
        v4u pa[8];
#pragma unroll
        for (int i = 0; i < 8; ++i) pa[i] = diag(pd[8 * hf + i]);
#pragma unroll
        for (int i = 0; i < 8; ++i) asm volatile("" : "+v"(pa[i]));
        if (hf == 1 && n == NTILE - 1) {
          wait4(&sm.ctl[VDONE]);
#pragma unroll
          for (int w = 0; w < NCONS; ++w)
            if (w != LASTC) O1 += *(const v4f*)&sm.qpart[w][8 * c + 4 * (t & 1)];
          asm volatile("" : "+v"(O1));
        }
        while (lds_ld(&sm.ctl[LANDED]) < (unsigned)(2 * n + 1 + hf)) {}
        asm volatile("" ::: "memory");
        v4u Vr[8];
#pragma unroll
        for (int i = 0; i < 4; ++i) { Vr[i] = *(const v4u*)(tb + hf * 8192 + i * 256); Vr[4 + i] = *(const v4u*)(tb + hf * 8192 + 4096 + i * 256); }
#pragma unroll
        for (int i = 0; i < 4; ++i) { O0 = mfma16(pa[i], Vr[i], O0); O1 = mfma16(pa[4 + i], Vr[4 + i], O1); }
      }
      asm volatile("s_waitcnt lgkmcnt(0)" ::: "memory");
      lds_st(&sm.ctl[FREED + ci], n + 1);
      slot += NCONS; slot = slot >= NSLOT ? slot - NSLOT : slot;
    }
    O0 += O1;
    if (ci == LASTC) {
      const __amdgpu_buffer_rsrc_t ro = __builtin_amdgcn_make_buffer_rsrc(ws + WS_OBOX + (size_t)(h * NB + b) * OBOX_B, 0, OBOX_B, 0x00020000);
      const v4u g = {cvtpk(O0[0], O0[1]), tag, cvtpk(O0[2], O0[3]), tag};
      if (lane < 32) __builtin_amdgcn_raw_buffer_store_b128(g, ro, (2 * c + t) * 16, 0, 16 + (O1NT & 1) * 2);
      STAMP(2);
    } else {
      if (lane < 32) *(v4f*)&sm.qpart[ci][8 * c + 4 * t] = O0;
      asm volatile("s_waitcnt lgkmcnt(0)" ::: "memory");
      lds_st(&sm.ctl[VDONE + ci], 1u);
    }
    gw = -1;
  } else {
    const int j = wave - 4;
    wg_barrier();
    const uint16_t* xr = x + b * DM;
    const v4u xa = ldnt(xr + 8 * lane), xb = ldnt(xr + 512 + 8 * c);
    float m1, den1, xv[16];
    unsigned tag;
    {
      v4u xq[5];
#pragma unroll
      for (int n = 0; n < 5; ++n) xq[n] = ldnt(xr + 160 * j + 40 * t + 8 * n);
      unsigned ep = 0;
      if (j == 0) ep = __hip_atomic_load(epoch_p, __ATOMIC_RELAXED, __HIP_MEMORY_SCOPE_AGENT);
      v4u W[40];
      const char* p = wq + (size_t)(160 * j + 40 * t) * (NH * DH * 2) + h * (DH * 2) + c * 16;
#pragma unroll
      for (int i = 0; i < 40; ++i) W[i] = (ABL & 1) ? dmask : ldnt(p + (size_t)i * (NH * DH * 2));
      __builtin_amdgcn_sched_barrier(0);
#pragma unroll
      for (int i = 0; i < 4; ++i) { xv[2 * i] = bflo(xa[i]); xv[2 * i + 1] = bfhi(xa[i]); xv[8 + 2 * i] = bflo(xb[i]); xv[9 + 2 * i] = bfhi(xb[i]); }
      ln_stats<16, false>(xv, lane < 16, m1, den1);
      v4f Q0 = {0.f, 0.f, 0.f, 0.f}, Q1 = {0.f, 0.f, 0.f, 0.f};
#pragma unroll
      for (int i = 0; i < 20; ++i) {
        const unsigned w = xq[i >> 2][i & 3];
        const float e0 = rbf(bflo(w) - m1) * den1, e1 = rbf(bfhi(w) - m1) * den1;
        Q0 = mfma16(diag(cvtpk(e0, e0)), W[2 * i], Q0);
        Q1 = mfma16(diag(cvtpk(e1, e1)), W[2 * i + 1], Q1);
      }
      Q0 += Q1;
      if (lane < 32) *(v4f*)&sm.qpart[j][8 * c + 4 * t] = Q0;
      if (j == 0) lds_st(&sm.ctl[TAG], ep + 1u);
      asm volatile("s_waitcnt lgkmcnt(0)" ::: "memory");
      lds_st(&sm.ctl[QFLAG + j], 1u);
      if (wave == 4) STAMP(8);
    }
    if (!(ABL & 8)) {
      const char* wb = wo + (size_t)(DH * h + 32 * j) * (DM * 2) + b * 160;
      const unsigned w0 = (unsigned)(size_t)(LAS uint16_t*)sm.wblk + j * 5120;
#pragma unroll
      for (int p = 0; p < 5; ++p) {
        const unsigned id = 64 * p + lane, row = id / 10;
        dma1(w0 + p * 1024, wb, row * (DM * 2) + (id - 10 * row) * 16);
      }
    }
    if (!(ABL & 4)) {
      unsigned r1[7], r2[7], r3[7];
#pragma unroll
      for (int i = 0; i < 7; ++i) {
        const int f = 256 * i + 64 * (i == 6 && j == 3 ? 0 : j) + lane;
        r1[i] = ldnt16(w1 + f); r2[i] = ldnt16(b1 + f); r3[i] = ldnt16(w2 + f);
      }
      const unsigned xcol = ldnt16(xr + COLS * h + (lane < COLS ? lane : 0));
      const unsigned bb = ldnt16(b2);
      r3[6] = j == 3 ? 0u : r3[6];
      float gw1[7], gb1[7], gw2[7];
#pragma unroll
      for (int i = 0; i < 7; ++i) { gw1[i] = bflo(r1[i]); gb1[i] = bflo(r2[i]); gw2[i] = bflo(r3[i]); }
      float m2, den2;
#pragma unroll
      for (int i = 0; i < 16; ++i) xv[i] = ln_apply(xv[i], m1, den1);
      ln_stats<16, false>(xv, lane < 16, m2, den2);
      const float hv = ln_apply(ln_apply(bflo(xcol), m1, den1), m2, den2);
      float gsum = 0.f;
#pragma unroll 2
      for (int col = 0; col < COLS; ++col) {
        const float hc = rdlane(hv, col);
        float acc = 0.f;
#pragma unroll
        for (int i = 0; i < 7; ++i) {
          const float u = rbf(rbf(hc * gw1[i]) + gb1[i]);
          const float z = u * fmaf(u * u, 0.044715f * 0.7978845608028654f * 2.f * LOG2E, 0.7978845608028654f * 2.f * LOG2E);
          const float ge = rbf(u * __builtin_amdgcn_rcpf(1.f + __builtin_amdgcn_exp2f(-z)));
          acc = fmaf(ge, gw2[i], acc);
        }
        acc = wave_sum(acc);
        gsum = lane == col ? acc : gsum;
      }
      if (lane < COLS) sm.gpart[j][lane] = gsum;
      if (j == 0) {
        if (lane < COLS) ((uint16_t*)sm.xfin)[lane] = (uint16_t)xcol;
        if (lane == 0) sm.xfin[10] = bb;
      }
    }
    asm volatile("s_waitcnt vmcnt(0) lgkmcnt(0)" ::: "memory");
    lds_st(&sm.ctl[GDONE + j], 1u);
    wait4(&sm.ctl[GDONE]);
    tag = sm.ctl[TAG];
    v4u Wt[2][4];
    {
      const uint16_t* wl = sm.wblk + 8 * t * 80 + c;
#pragma unroll
      for (int e = 0; e < 2; ++e)
#pragma unroll
        for (int s = 0; s < 4; ++s)
#pragma unroll
          for (int i = 0; i < 4; ++i) {
            const uint16_t* q = wl + (32 * s + 2 * i) * 80 + (e ? 64 : 16 * j);
            Wt[e][s][i] = (unsigned)q[0] | ((unsigned)q[80] << 16);
          }
#pragma unroll
      for (int s = 0; s < 4; ++s) asm volatile("" : "+v"(Wt[0][s]), "+v"(Wt[1][s]));
    }
    if (wave == 4) STAMP(3);
    while (lds_ld(&sm.ctl[LANDED]) < (unsigned)(2 * NTILE)) __builtin_amdgcn_s_sleep(2);
    if (O1DELAY) __builtin_amdgcn_s_sleep(O1DELAY);
    const char* gp = ws + WS_OBOX + (size_t)(h * NB + (c & 7)) * OBOX_B + (2 * t + (c >> 3)) * 16;
    v4u r[4];
#pragma unroll
    for (int s = 0; s < 4; ++s) r[s] = v4u{0u, 0u, 0u, 0u};
    {
      bool ok = false;
      for (unsigned spins = 0; spins < ((ABL & 64) ? 0 : SPIN_CAP); ++spins) {
        if (!ok) {
          asm volatile("global_load_dwordx4 %0, %4, off " O1POL "\n\tglobal_load_dwordx4 %1, %4, off offset:128 " O1POL "\n\t"
                       "global_load_dwordx4 %2, %4, off offset:256 " O1POL "\n\tglobal_load_dwordx4 %3, %4, off offset:384 " O1POL "\n\ts_waitcnt vmcnt(0)"
                       : "+v"(r[0]), "+v"(r[1]), "+v"(r[2]), "+v"(r[3]) : "v"(gp) : "memory");
          ok = r[0][1] == tag && r[0][3] == tag && r[1][1] == tag && r[1][3] == tag && r[2][1] == tag && r[2][3] == tag && r[3][1] == tag && r[3][3] == tag;
        }
        const unsigned long long bm = __ballot(!ok);
        if (!bm) break;
        if (__builtin_popcountll(bm) > 16) __builtin_amdgcn_s_sleep(8); else __builtin_amdgcn_s_sleep(1);
      }
    }
    if (wave == 4) STAMP(7);
    v4f D0 = {0.f, 0.f, 0.f, 0.f}, D1 = {0.f, 0.f, 0.f, 0.f};
#pragma unroll
    for (int s = 0; s < 4; ++s) {
      const unsigned y0 = __builtin_amdgcn_mov_dpp(r[s][0], 0x128, 0xf, 0xf, true), y1 = __builtin_amdgcn_mov_dpp(r[s][2], 0x128, 0xf, 0xf, true);
      const v4u ob = c < 8 ? v4u{r[s][0], r[s][2], y0, y1} : v4u{y0, y1, r[s][0], r[s][2]};
      D0 = mfma16(Wt[0][s], ob, D0);
      if (j == 3) D1 = mfma16(Wt[1][s], ob, D1);
    }
    {
      const __amdgpu_buffer_rsrc_t rs = __builtin_amdgcn_make_buffer_rsrc(ws + WS_SBOX, 0, NB * SBOX_B, 0x00020000);
      auto send = [&](const v4f& D, int ct) {
        const v4u g = {__float_as_uint(c < 8 ? D[0] : D[2]), tag, __float_as_uint(c < 8 ? D[1] : D[3]), tag};
        __builtin_amdgcn_raw_buffer_store_b128(g, rs, (((c & 7) * 40 + 5 * b + ct) * NH + h) * 128 + t * 32 + (c >> 3) * 16, 0, 16 + (S2NT & 1) * 2);
      };
      send(D0, j);
      if (j == 3) send(D1, 4);
    }
    if (wave == 4) STAMP(4);
    gw = j;
  }
  if (gw < 0) return;
  {
    wait4(&sm.ctl[GDONE]);
    const unsigned tag = sm.ctl[TAG];
    const int cp = 2 * gw + (lane >> 5), prod = lane & 31;
    unsigned xw = sm.xfin[cp];
    float y0 = 0.f, y1 = 0.f;
#pragma unroll
    for (int w = 0; w < 4; ++w) { y0 += sm.gpart[w][2 * cp]; y1 += sm.gpart[w][2 * cp + 1]; }
    const float bb = bflo(sm.xfin[10]);
    y0 = rbf(rbf(y0) + bb); y1 = rbf(rbf(y1) + bb);
    float g0 = rbf(y0 / (1.f + __expf(-y0))), g1 = rbf(y1 / (1.f + __expf(-y1)));
    asm volatile("" : "+v"(g0), "+v"(g1), "+v"(xw));
    const int col = COLS * h + 2 * cp;
    const char* gp = ws + WS_SBOX + (size_t)b * SBOX_B + ((col >> 4) * NH + prod) * 128 + (col & 15) * 8;
    if (S2DELAY) __builtin_amdgcn_s_sleep(S2DELAY);
    v4u g = {0u, 0u, 0u, 0u};
    {
      bool ok = false, full = true;
      unsigned miss = ~0u;
      for (unsigned spins = 0; spins < ((ABL & 64) ? 0 : SPIN_CAP); ++spins) {
        const bool act = !ok && (full || __builtin_popcount(miss & ((1u << prod) - 1u)) < 2);
        if (act) {
          asm volatile("global_load_dwordx4 %0, %1, off " S2POL "\n\ts_waitcnt vmcnt(0)" : "+v"(g) : "v"(gp) : "memory");
          ok = g[1] == tag && g[3] == tag;
        }
        const unsigned long long bm = __ballot(!ok);
        if (!bm) break;
        const unsigned nm = (unsigned)bm | (unsigned)(bm >> 32);
        full = nm != miss;
        miss = nm;
        if (!full) { if (__builtin_popcount(miss) > 4) __builtin_amdgcn_s_sleep(8); else __builtin_amdgcn_s_sleep(1); }
      }
    }
    const float s0 = rbf(half32_sum(__uint_as_float(g[0]))), s1 = rbf(half32_sum(__uint_as_float(g[2])));
    const unsigned res = cvtpk(bflo(xw) + rbf(s0 * g0), bfhi(xw) + rbf(s1 * g1));
    {
      const __amdgpu_buffer_rsrc_t rout = __builtin_amdgcn_make_buffer_rsrc(out, 0, NB * DM * 2, 0x00020000);
      if (prod == 0) __builtin_amdgcn_raw_buffer_store_b32(res, rout, (b * DM + COLS * h + 2 * cp) * 2, 0, OUTPOL);
    }
    if (bid == 0 && tid == 0) __hip_atomic_store(epoch_p, tag, __ATOMIC_RELAXED, __HIP_MEMORY_SCOPE_AGENT);
  }
}

extern "C" void kernel_launch(void* const* d_in, const int* in_sizes, int n_in, void* d_out, int out_size, void* d_ws,
                              size_t ws_size, hipStream_t stream) {
  fused_decode<<<256, 512, 0, stream>>>((const uint16_t*)d_in[0], (const char*)d_in[1], (const char*)d_in[2],
                                         (const char*)d_in[3], (const char*)d_in[4], (const uint16_t*)d_in[6],
                                         (const uint16_t*)d_in[7], (const uint16_t*)d_in[8], (const uint16_t*)d_in[9],
                                         (uint16_t*)d_out, (char*)d_ws);
}
```

```cpp
#include <hip/hip_runtime.h>
#include <hip/hip_bf16.h>
#include <stdint.h>
#include <stdio.h>

#define GAS __attribute__((address_space(1)))
#define LAS __attribute__((address_space(3)))
typedef unsigned v4u __attribute__((ext_vector_type(4)));
typedef unsigned v2u __attribute__((ext_vector_type(2)));
typedef float v4f __attribute__((ext_vector_type(4)));
typedef float v2f __attribute__((ext_vector_type(2)));
typedef __bf16 bf16x8 __attribute__((ext_vector_type(8)));
typedef __bf16 bf16x2 __attribute__((ext_vector_type(2)));

#define DMIN 18
#define DSTEP 2
#define NLEV 6
#define LSH 6
#define TAU 856
#define TOFF 300
#define OUTPOL 0
#define O1NT 0
#define S2NT 0
#define O1DELAY 0
#define S2DELAY 0
#define O1POL "sc1"
#define S2POL "sc1"
#define ABL 0
#define NPRE 3

constexpr int NB = 8, DM = 640, NH = 32, SK = 4096, DH = 128, FF = 1728;
constexpr int COLS = DM / NH;
constexpr float LOG2E = 1.4426950408889634f;
constexpr float SCALE = 0.08838834764831845f;
constexpr unsigned SPIN_CAP = 1u << 20;

constexpr size_t WS_OBOX = 4096, OBOX_B = 512, WS_SBOX = WS_OBOX + 256 * OBOX_B, SBOX_B = 40 * NH * 128;

__device__ __forceinline__ v4f mfma16(v4u a, v4u b, v4f c) {
  return __builtin_amdgcn_mfma_f32_16x16x32_bf16(__builtin_bit_cast(bf16x8, a), __builtin_bit_cast(bf16x8, b), c, 0, 0, 0);
}
__device__ __forceinline__ unsigned cvtpk(float lo, float hi) {
  bf16x2 r = {(__bf16)lo, (__bf16)hi};
  return __builtin_bit_cast(unsigned, r);
}
__device__ __forceinline__ float bflo(unsigned u) { return __builtin_bit_cast(float, u << 16); }
__device__ __forceinline__ float bfhi(unsigned u) { return __builtin_bit_cast(float, u & 0xffff0000u); }
__device__ __forceinline__ float rbf(float x) { return (float)(__bf16)x; }
__device__ __forceinline__ v4u ldnt(const void* p) { return __builtin_nontemporal_load((const GAS v4u*)p); }
__device__ __forceinline__ unsigned ldnt16(const uint16_t* p) { return __builtin_nontemporal_load((const GAS uint16_t*)p); }
template <int D> __device__ __forceinline__ void dma4(unsigned lds, const char* base, unsigned o0, unsigned o1, unsigned o2, unsigned o3) {
  asm volatile("s_mov_b32 m0, %0\n\t"
               "s_waitcnt vmcnt(%6)\n\tglobal_load_lds_dwordx4 %2, %1 nt\n\t"
               "s_waitcnt vmcnt(%6)\n\tglobal_load_lds_dwordx4 %3, %1 offset:1024 nt\n\t"
               "s_waitcnt vmcnt(%6)\n\tglobal_load_lds_dwordx4 %4, %1 offset:2048 nt\n\t"
               "s_waitcnt vmcnt(%6)\n\tglobal_load_lds_dwordx4 %5, %1 offset:3072 nt"
               :: "s"(lds), "s"(base), "v"(o0), "v"(o1), "v"(o2), "v"(o3), "i"(D - 1) : "memory");
}
__device__ __forceinline__ void dma1(unsigned lds, const char* base, unsigned o) {
  asm volatile("s_mov_b32 m0, %0\n\ts_nop 0\n\tglobal_load_lds_dwordx4 %2, %1 nt" :: "s"(lds), "s"(base), "v"(o) : "memory");
}
__device__ __forceinline__ unsigned lds_ld(const unsigned* p) { return __builtin_amdgcn_readfirstlane(*(const volatile LAS unsigned*)p); }
__device__ __forceinline__ void lds_st(unsigned* p, unsigned v) { *(volatile LAS unsigned*)p = v; }

template <int CTRL> __device__ __forceinline__ float dpp(float x) {
  return __builtin_bit_cast(float, __builtin_amdgcn_mov_dpp(__builtin_bit_cast(int, x), CTRL, 0xf, 0xf, true));
}
constexpr int XOR1 = 0xB1, XOR2 = 0x4E, XOR7 = 0x141, XOR15 = 0x140;
__device__ __forceinline__ float swap16_sum(float x) {
  auto s = __builtin_amdgcn_permlane16_swap(__float_as_uint(x), __float_as_uint(x), false, false);
  return __uint_as_float(s[0]) + __uint_as_float(s[1]);
}
__device__ __forceinline__ float swap32_sum(float x) {
  auto s = __builtin_amdgcn_permlane32_swap(__float_as_uint(x), __float_as_uint(x), false, false);
  return __uint_as_float(s[0]) + __uint_as_float(s[1]);
}
__device__ __forceinline__ float swap16_max(float x) {
  auto s = __builtin_amdgcn_permlane16_swap(__float_as_uint(x), __float_as_uint(x), false, false);
  return fmaxf(__uint_as_float(s[0]), __uint_as_float(s[1]));
}
__device__ __forceinline__ float swap32_max(float x) {
  auto s = __builtin_amdgcn_permlane32_swap(__float_as_uint(x), __float_as_uint(x), false, false);
  return fmaxf(__uint_as_float(s[0]), __uint_as_float(s[1]));
}
__device__ __forceinline__ float row16_sum(float x) {
  x += dpp<XOR1>(x); x += dpp<XOR2>(x); x += dpp<XOR7>(x); x += dpp<XOR15>(x); return x;
}
__device__ __forceinline__ float half32_sum(float x) { return swap16_sum(row16_sum(x)); }
__device__ __forceinline__ float wave_sum(float x) { return swap32_sum(half32_sum(x)); }

__device__ __forceinline__ float wave_max(float x) {
  x = fmaxf(x, dpp<XOR1>(x)); x = fmaxf(x, dpp<XOR2>(x)); x = fmaxf(x, dpp<XOR7>(x)); x = fmaxf(x, dpp<XOR15>(x));
  return swap32_max(swap16_max(x));
}
__device__ __forceinline__ void wg_barrier() {
  asm volatile("s_waitcnt lgkmcnt(0)" ::: "memory");
  __builtin_amdgcn_s_barrier();
  asm volatile("" ::: "memory");
}

__device__ __forceinline__ float ln_apply(float x, float m, float rden) { return rbf(rbf(x - m) * rden); }
template <int N, bool HALF>
__device__ __forceinline__ void ln_stats(const float (&v)[N], bool tail, float& m, float& den) {
  float s = 0.f, s2 = 0.f;
#pragma unroll
  for (int i = 0; i < N - 8; ++i) s += v[i];
#pragma unroll
  for (int i = N - 8; i < N; ++i) s2 += v[i];
  s += tail ? s2 : 0.f;
  s = HALF ? half32_sum(s) : wave_sum(s);
  const float mean = s * (1.0f / DM);
  float q = 0.f, q2 = 0.f;
#pragma unroll
  for (int i = 0; i < N - 8; ++i) { const float d = v[i] - mean; q = fmaf(d, d, q); }
#pragma unroll
  for (int i = N - 8; i < N; ++i) { const float d = v[i] - mean; q2 = fmaf(d, d, q2); }
  q += tail ? q2 : 0.f;
  q = HALF ? half32_sum(q) : wave_sum(q);
  const float var = rbf(q * (1.0f / DM));
  m = rbf(mean);
  den = __builtin_amdgcn_rcpf(rbf(__builtin_sqrtf(rbf(var + 1e-5f))));
}
__device__ __forceinline__ float rdlane(float x, int l) { return __builtin_bit_cast(float, __builtin_amdgcn_readlane(__builtin_bit_cast(int, x), l)); }

#define STAMP(k) do {} while (0)
constexpr int NSLOT = 8, TILE_B = 16384, NKT = SK / 64, NTILE = 2 * NKT, NCONS = 3;
constexpr int LASTC = (NTILE - 1) % NCONS;
struct Smem {
  unsigned char ring[NSLOT * TILE_B];
  uint16_t wblk[DH * 80];
  uint16_t sc[SK];
  float qpart[4][DH];
  unsigned qbf[NCONS][64];
  float ml[NCONS][2];
  float pad[2];
  float gpart[4][32];
  unsigned xfin[16];
  unsigned ctl[32];
};
constexpr int QFLAG = 0, WLAND = 4, FREED = 8, LANDED = 12, TAG = 15, KDONE = 16, VDONE = 20, GDONE = 24;

__device__ __forceinline__ void wait4(const unsigned* p) {
  for (;;) {
    const v4u f = *(const volatile LAS v4u*)p;
    if (__builtin_amdgcn_readfirstlane(f[0] & f[1] & f[2] & f[3])) break;
    __builtin_amdgcn_s_sleep(1);
  }
  asm volatile("" ::: "memory");
}

template <int D, typename F>
__device__ __forceinline__ void tile(int n, unsigned slot, const char* src, const unsigned (&of)[4], F& publish) {
  constexpr int PH = ((D + 3) / 4 + 1) & 1;
  dma4<D>(slot, src, of[0], of[1], of[2], of[3]);
  if (PH == 0) publish((16 * n + 4 - D) >> 3);
  dma4<D>(slot + 4096, src + 4096, of[0], of[1], of[2], of[3]);
  if (PH == 1) publish((16 * n + 8 - D) >> 3);
  dma4<D>(slot + 8192, src + 8192, of[0], of[1], of[2], of[3]);
  if (PH == 0) publish((16 * n + 12 - D) >> 3);
  dma4<D>(slot + 12288, src + 12288, of[0], of[1], of[2], of[3]);
  if (PH == 1) publish((16 * n + 16 - D) >> 3);
}

__global__ void __launch_bounds__(512, 2)
fused_decode(const uint16_t* __restrict__ x, const char* __restrict__ kc, const char* __restrict__ vc,
             const char* __restrict__ wq, const char* __restrict__ wo, const uint16_t* __restrict__ w1,
             const uint16_t* __restrict__ b1, const uint16_t* __restrict__ w2, const uint16_t* __restrict__ b2,
             uint16_t* __restrict__ out, char* ws) {
  __shared__ __attribute__((aligned(16))) Smem sm;
  const int tid = threadIdx.x, lane = tid & 63, wave = __builtin_amdgcn_readfirstlane(tid >> 6);
  const int bid = blockIdx.x;
  const int h = (bid & 7) * 4 + ((bid >> 3) & 3), b = bid >> 5;
  const int t = lane >> 4, c = lane & 15;
  v4u dmask;
#pragma unroll
  for (int i = 0; i < 4; ++i) dmask[i] = ((c & 7) >> 1) == i ? ((c & 1) ? 0xffff0000u : 0x0000ffffu) : 0u;
  auto diag = [&](unsigned dup) { v4u a; a[0] = dup & dmask[0]; a[1] = dup & dmask[1]; a[2] = dup & dmask[2]; a[3] = dup & dmask[3]; return a; };
  GAS unsigned* epoch_p = (GAS unsigned*)ws;
  int gw;

  if (wave == 0) {
    STAMP(0);
    __builtin_amdgcn_s_setprio(3);
    const size_t hb = ((size_t)(b * NH + h)) * (SK * DH * 2);
    const char* src = kc + hb;
    unsigned of[4];
#pragma unroll
    for (int i = 0; i < 4; ++i) of[i] = t * 256 + ((c ^ (4 * i + t)) * 16);
    const unsigned ring0 = (unsigned)(size_t)(LAS unsigned char*)sm.ring;
    unsigned slot = ring0;
    int pub = 0;
    auto publish = [&](int v) { if (v > pub) { pub = v; lds_st(&sm.ctl[LANDED], (unsigned)v); } };
    dma4<63>(slot, src, of[0], of[1], of[2], of[3]);
    dma4<63>(slot + 4096, src + 4096, of[0], of[1], of[2], of[3]);
    wg_barrier();
    dma4<63>(slot + 8192, src + 8192, of[0], of[1], of[2], of[3]);
    dma4<63>(slot + 12288, src + 12288, of[0], of[1], of[2], of[3]);
    const unsigned tstart = (unsigned)__builtin_amdgcn_s_memrealtime();
    int cons = (NCONS - NSLOT % NCONS) % NCONS;
    for (int n = 1; n < NTILE; ++n) {
      src += TILE_B;
      if (n == NKT) { src = vc + hb; of[0] = of[1] = of[2] = of[3] = lane * 16; }
      slot += TILE_B; if (slot == ring0 + NSLOT * TILE_B) slot = ring0;
      cons = cons == NCONS - 1 ? 0 : cons + 1;
      if (n >= NSLOT) {
        const unsigned need = n - NSLOT + 1;
        if (lds_ld(&sm.ctl[FREED + cons]) < need) {
          asm volatile("s_waitcnt vmcnt(0)" ::: "memory");
          publish(2 * n);
          while (lds_ld(&sm.ctl[FREED + cons]) < need) __builtin_amdgcn_s_sleep(1);
        }
      }
      const int lev = ((int)((unsigned)__builtin_amdgcn_s_memrealtime() - tstart) - TOFF - ((n * TAU) >> 4)) >> LSH;
      if (lev <= 0) tile<DMIN>(n, slot, src, of, publish);
      else if (lev == 1 || NLEV == 2) tile<DMIN + DSTEP>(n, slot, src, of, publish);
      else if (lev == 2 || NLEV == 3) tile<DMIN + 2 * DSTEP>(n, slot, src, of, publish);
      else if (lev == 3 || NLEV == 4) tile<DMIN + 3 * DSTEP>(n, slot, src, of, publish);
      else if (lev == 4 || NLEV == 5) tile<DMIN + 4 * DSTEP>(n, slot, src, of, publish);
      else tile<DMIN + 5 * DSTEP>(n, slot, src, of, publish);
    }
    asm volatile("s_waitcnt vmcnt(24)" ::: "memory");
    publish(2 * NTILE - 3);
    asm volatile("s_waitcnt vmcnt(16)" ::: "memory");
    publish(2 * NTILE - 2);
    asm volatile("s_waitcnt vmcnt(8)" ::: "memory");
    publish(2 * NTILE - 1);
    asm volatile("s_waitcnt vmcnt(0)" ::: "memory");
    publish(2 * NTILE);
    STAMP(6);
    __builtin_amdgcn_s_setprio(0);
    gw = 4;
  } else if (wave < 4) {
    const int ci = wave - 1;
    if (wave == 1 && lane < 32) sm.ctl[lane] = (lane == KDONE + 3 || lane == VDONE + 3 || lane == VDONE + LASTC) ? 1u : 0u;
    wg_barrier();
    unsigned kof[4];
#pragma unroll
    for (int s = 0; s < 4; ++s) kof[s] = c * 256 + (((4 * s + t) ^ c) * 16);
    int slot = ci, n = ci;
    auto fetchK = [&](v4u (&Kr)[16]) {
      const unsigned char* tb = sm.ring + slot * TILE_B;
      while (lds_ld(&sm.ctl[LANDED]) < (unsigned)(2 * n + 2)) __builtin_amdgcn_s_sleep(1);
      asm volatile("" ::: "memory");
#pragma unroll
      for (int u = 0; u < 4; ++u)
#pragma unroll
        for (int s = 0; s < 4; ++s) Kr[4 * u + s] = *(const v4u*)(tb + u * 4096 + kof[s]);
      asm volatile("s_waitcnt lgkmcnt(0)" ::: "memory");
      lds_st(&sm.ctl[FREED + ci], n + 1);
#pragma unroll
      for (int i = 0; i < 16; ++i) asm volatile("" : "+v"(Kr[i]));
      slot += NCONS; slot = slot >= NSLOT ? slot - NSLOT : slot;
      n += NCONS;
    };
    v4u Kp[NPRE][16];
#pragma unroll
    for (int k = 0; k < NPRE; ++k) fetchK(Kp[k]);
    wait4(&sm.ctl[QFLAG]);
    if (wave == 1) STAMP(1);
    v4u qf[4];
    {
      float q0 = 0.f, q1 = 0.f;
#pragma unroll
      for (int j = 0; j < 4; ++j) { const v2f p = *(const v2f*)&sm.qpart[j][2 * lane]; q0 += p[0]; q1 += p[1]; }
      sm.qbf[ci][lane] = cvtpk(q0, q1);
      asm volatile("s_waitcnt lgkmcnt(0)" ::: "memory");
#pragma unroll
      for (int s = 0; s < 4; ++s) qf[s] = *(const v4u*)&sm.qbf[ci][16 * s + 4 * t];
    }
    float m = -INFINITY, l = 0.f;
    auto scoreK = [&](const v4u (&Kr)[16], int tile) {
      v4f S[4];
#pragma unroll
      for (int u = 0; u < 4; ++u) S[u] = v4f{0.f, 0.f, 0.f, 0.f};
#pragma unroll
      for (int s = 0; s < 4; ++s)
#pragma unroll
        for (int u = 0; u < 4; ++u) S[u] = mfma16(qf[s], Kr[4 * u + s], S[u]);
      const float sr = t == 0 ? S[0][0] : t == 1 ? S[1][0] : t == 2 ? S[2][0] : S[3][0];
      const unsigned sb = cvtpk(rbf(sr) * SCALE, 0.f);
      sm.sc[64 * tile + lane] = (uint16_t)sb;
      const float sv = bflo(sb) * LOG2E;
      const float mn = fmaxf(m, sv);
      l = fmaf(l, __builtin_amdgcn_exp2f(m - mn), __builtin_amdgcn_exp2f(sv - mn));
      m = mn;
    };
#pragma unroll
    for (int k = 0; k < NPRE; ++k) scoreK(Kp[k], ci + NCONS * k);
    while (n < NKT) {
      v4u Kr[16];
      fetchK(Kr);
      scoreK(Kr, n - NCONS);
    }
    {
      const float mw = wave_max(m);
      const float lw = wave_sum(l * __builtin_amdgcn_exp2f(m - mw));
      if (lane == 0) { sm.ml[ci][0] = mw; sm.ml[ci][1] = lw; }
      asm volatile("s_waitcnt lgkmcnt(0)" ::: "memory");
      lds_st(&sm.ctl[KDONE + ci], 1u);
      wait4(&sm.ctl[KDONE]);
    }
    float M = -INFINITY, L = 0.f;
#pragma unroll
    for (int w = 0; w < NCONS; ++w) M = fmaxf(M, sm.ml[w][0]);
#pragma unroll
    for (int w = 0; w < NCONS; ++w) L = fmaf(sm.ml[w][1], __builtin_amdgcn_exp2f(sm.ml[w][0] - M), L);
    const float invL = 1.0f / L;
    const unsigned tag = sm.ctl[TAG];
    if (wave == 1) STAMP(13);
    v4f O0 = {0.f, 0.f, 0.f, 0.f}, O1 = {0.f, 0.f, 0.f, 0.f};
    const unsigned vof = t * 1024 + c * 16;
    for (; n < NTILE; n += NCONS) {
      const unsigned char* tb = sm.ring + slot * TILE_B + vof;
      unsigned pd[16];
#pragma unroll
      for (int qd = 0; qd < 4; ++qd) {
        const v2u sv = *(const v2u*)&sm.sc[64 * (n - NKT) + 16 * qd + 4 * t];
#pragma unroll
        for (int i = 0; i < 2; ++i) {
          const float p0 = __builtin_amdgcn_exp2f(fmaf(bflo(sv[i]), LOG2E, -M)) * invL, p1 = __builtin_amdgcn_exp2f(fmaf(bfhi(sv[i]), LOG2E, -M)) * invL;
          pd[4 * qd + 2 * i] = cvtpk(p0, p0); pd[4 * qd + 2 * i + 1] = cvtpk(p1, p1);
        }
      }
#pragma unroll
      for (int hf = 0; hf < 2; ++hf) {
        v4u pa[8];
#pragma unroll
        for (int i = 0; i < 8; ++i) pa[i] = diag(pd[8 * hf + i]);
#pragma unroll
        for (int i = 0; i < 8; ++i) asm volatile("" : "+v"(pa[i]));
        if (hf == 1 && n == NTILE - 1) {
          wait4(&sm.ctl[VDONE]);
#pragma unroll
          for (int w = 0; w < NCONS; ++w)
            if (w != LASTC) O1 += *(const v4f*)&sm.qpart[w][8 * c + 4 * (t & 1)];
          asm volatile("" : "+v"(O1));
        }
        while (lds_ld(&sm.ctl[LANDED]) < (unsigned)(2 * n + 1 + hf)) {}
        asm volatile("" ::: "memory");
        v4u Vr[8];
#pragma unroll
        for (int i = 0; i < 4; ++i) { Vr[i] = *(const v4u*)(tb + hf * 8192 + i * 256); Vr[4 + i] = *(const v4u*)(tb + hf * 8192 + 4096 + i * 256); }
#pragma unroll
        for (int i = 0; i < 4; ++i) { O0 = mfma16(pa[i], Vr[i], O0); O1 = mfma16(pa[4 + i], Vr[4 + i], O1); }
      }
      asm volatile("s_waitcnt lgkmcnt(0)" ::: "memory");
      lds_st(&sm.ctl[FREED + ci], n + 1);
      slot += NCONS; slot = slot >= NSLOT ? slot - NSLOT : slot;
    }
    O0 += O1;
    if (ci == LASTC) {
      const __amdgpu_buffer_rsrc_t ro = __builtin_amdgcn_make_buffer_rsrc(ws + WS_OBOX + (size_t)(h * NB + b) * OBOX_B, 0, OBOX_B, 0x00020000);
      const v4u g = {cvtpk(O0[0], O0[1]), tag, cvtpk(O0[2], O0[3]), tag};
      if (lane < 32) __builtin_amdgcn_raw_buffer_store_b128(g, ro, (2 * c + t) * 16, 0, 16 + (O1NT & 1) * 2);
      STAMP(2);
    } else {
      if (lane < 32) *(v4f*)&sm.qpart[ci][8 * c + 4 * t] = O0;
      asm volatile("s_waitcnt lgkmcnt(0)" ::: "memory");
      lds_st(&sm.ctl[VDONE + ci], 1u);
    }
    gw = -1;
  } else {
    const int j = wave - 4;
    wg_barrier();
    const uint16_t* xr = x + b * DM;
    const v4u xa = ldnt(xr + 8 * lane), xb = ldnt(xr + 512 + 8 * c);
    float m1, den1, xv[16];
    unsigned tag;
    {
      v4u xq[5];
#pragma unroll
      for (int n = 0; n < 5; ++n) xq[n] = ldnt(xr + 160 * j + 40 * t + 8 * n);
      unsigned ep = 0;
      if (j == 0) ep = __hip_atomic_load(epoch_p, __ATOMIC_RELAXED, __HIP_MEMORY_SCOPE_AGENT);
      v4u W[40];
      const char* p = wq + (size_t)(160 * j + 40 * t) * (NH * DH * 2) + h * (DH * 2) + c * 16;
#pragma unroll
      for (int i = 0; i < 40; ++i) W[i] = (ABL & 1) ? dmask : ldnt(p + (size_t)i * (NH * DH * 2));
      __builtin_amdgcn_sched_barrier(0);
#pragma unroll
      for (int i = 0; i < 4; ++i) { xv[2 * i] = bflo(xa[i]); xv[2 * i + 1] = bfhi(xa[i]); xv[8 + 2 * i] = bflo(xb[i]); xv[9 + 2 * i] = bfhi(xb[i]); }
      ln_stats<16, false>(xv, lane < 16, m1, den1);
      v4f Q0 = {0.f, 0.f, 0.f, 0.f}, Q1 = {0.f, 0.f, 0.f, 0.f};
#pragma unroll
      for (int i = 0; i < 20; ++i) {
        const unsigned w = xq[i >> 2][i & 3];
        const float e0 = rbf(bflo(w) - m1) * den1, e1 = rbf(bfhi(w) - m1) * den1;
        Q0 = mfma16(diag(cvtpk(e0, e0)), W[2 * i], Q0);
        Q1 = mfma16(diag(cvtpk(e1, e1)), W[2 * i + 1], Q1);
      }
      Q0 += Q1;
      if (lane < 32) *(v4f*)&sm.qpart[j][8 * c + 4 * t] = Q0;
      if (j == 0) lds_st(&sm.ctl[TAG], ep + 1u);
      asm volatile("s_waitcnt lgkmcnt(0)" ::: "memory");
      lds_st(&sm.ctl[QFLAG + j], 1u);
      if (wave == 4) STAMP(8);
    }
    if (!(ABL & 8)) {
      const char* wb = wo + (size_t)(DH * h + 32 * j) * (DM * 2) + b * 160;
      const unsigned w0 = (unsigned)(size_t)(LAS uint16_t*)sm.wblk + j * 5120;
#pragma unroll
      for (int p = 0; p < 5; ++p) {
        const unsigned id = 64 * p + lane, row = id / 10;
        dma1(w0 + p * 1024, wb, row * (DM * 2) + (id - 10 * row) * 16);
      }
    }
    if (!(ABL & 4)) {
      unsigned r1[7], r2[7], r3[7];
#pragma unroll
      for (int i = 0; i < 7; ++i) {
        const int f = 256 * i + 64 * (i == 6 && j == 3 ? 0 : j) + lane;
        r1[i] = ldnt16(w1 + f); r2[i] = ldnt16(b1 + f); r3[i] = ldnt16(w2 + f);
      }
      const unsigned xcol = ldnt16(xr + COLS * h + (lane < COLS ? lane : 0));
      const unsigned bb = ldnt16(b2);
      r3[6] = j == 3 ? 0u : r3[6];
      float gw1[7], gb1[7], gw2[7];
#pragma unroll
      for (int i = 0; i < 7; ++i) { gw1[i] = bflo(r1[i]); gb1[i] = bflo(r2[i]); gw2[i] = bflo(r3[i]); }
      float m2, den2;
#pragma unroll
      for (int i = 0; i < 16; ++i) xv[i] = ln_apply(xv[i], m1, den1);
      ln_stats<16, false>(xv, lane < 16, m2, den2);
      const float hv = ln_apply(ln_apply(bflo(xcol), m1, den1), m2, den2);
      float gsum = 0.f;
#pragma unroll 2
      for (int col = 0; col < COLS; ++col) {
        const float hc = rdlane(hv, col);
        float acc = 0.f;
#pragma unroll
        for (int i = 0; i < 7; ++i) {
          const float u = rbf(rbf(hc * gw1[i]) + gb1[i]);
          const float z = u * fmaf(u * u, 0.044715f * 0.7978845608028654f * 2.f * LOG2E, 0.7978845608028654f * 2.f * LOG2E);
          const float ge = rbf(u * __builtin_amdgcn_rcpf(1.f + __builtin_amdgcn_exp2f(-z)));
          acc = fmaf(ge, gw2[i], acc);
        }
        acc = wave_sum(acc);
        gsum = lane == col ? acc : gsum;
      }
      if (lane < COLS) sm.gpart[j][lane] = gsum;
      if (j == 0) {
        if (lane < COLS) ((uint16_t*)sm.xfin)[lane] = (uint16_t)xcol;
        if (lane == 0) sm.xfin[10] = bb;
      }
    }
    asm volatile("s_waitcnt vmcnt(0) lgkmcnt(0)" ::: "memory");
    lds_st(&sm.ctl[GDONE + j], 1u);
    wait4(&sm.ctl[GDONE]);
    tag = sm.ctl[TAG];
    v4u Wt[2][4];
    {
      const uint16_t* wl = sm.wblk + 8 * t * 80 + c;
#pragma unroll
      for (int e = 0; e < 2; ++e)
#pragma unroll
        for (int s = 0; s < 4; ++s)
#pragma unroll
          for (int i = 0; i < 4; ++i) {
            const uint16_t* q = wl + (32 * s + 2 * i) * 80 + (e ? 64 : 16 * j);
            Wt[e][s][i] = (unsigned)q[0] | ((unsigned)q[80] << 16);
          }
#pragma unroll
      for (int s = 0; s < 4; ++s) asm volatile("" : "+v"(Wt[0][s]), "+v"(Wt[1][s]));
    }
    if (wave == 4) STAMP(3);
    while (lds_ld(&sm.ctl[LANDED]) < (unsigned)(2 * NTILE)) __builtin_amdgcn_s_sleep(2);
    if (O1DELAY) __builtin_amdgcn_s_sleep(O1DELAY);
    const char* gp = ws + WS_OBOX + (size_t)(h * NB + (c & 7)) * OBOX_B + (2 * t + (c >> 3)) * 16;
    v4u r[4];
#pragma unroll
    for (int s = 0; s < 4; ++s) r[s] = v4u{0u, 0u, 0u, 0u};
    {
      bool ok = false;
      for (unsigned spins = 0; spins < ((ABL & 64) ? 0 : SPIN_CAP); ++spins) {
        if (!ok) {
          asm volatile("global_load_dwordx4 %0, %4, off " O1POL "\n\tglobal_load_dwordx4 %1, %4, off offset:128 " O1POL "\n\t"
                       "global_load_dwordx4 %2, %4, off offset:256 " O1POL "\n\tglobal_load_dwordx4 %3, %4, off offset:384 " O1POL "\n\ts_waitcnt vmcnt(0)"
                       : "+v"(r[0]), "+v"(r[1]), "+v"(r[2]), "+v"(r[3]) : "v"(gp) : "memory");
          ok = r[0][1] == tag && r[0][3] == tag && r[1][1] == tag && r[1][3] == tag && r[2][1] == tag && r[2][3] == tag && r[3][1] == tag && r[3][3] == tag;
        }
        const unsigned long long bm = __ballot(!ok);
        if (!bm) break;
        if (__builtin_popcountll(bm) > 16) __builtin_amdgcn_s_sleep(8); else __builtin_amdgcn_s_sleep(1);
      }
    }
    if (wave == 4) STAMP(7);
    v4f D0 = {0.f, 0.f, 0.f, 0.f}, D1 = {0.f, 0.f, 0.f, 0.f};
#pragma unroll
    for (int s = 0; s < 4; ++s) {
      const unsigned y0 = __builtin_amdgcn_mov_dpp(r[s][0], 0x128, 0xf, 0xf, true), y1 = __builtin_amdgcn_mov_dpp(r[s][2], 0x128, 0xf, 0xf, true);
      const v4u ob = c < 8 ? v4u{r[s][0], r[s][2], y0, y1} : v4u{y0, y1, r[s][0], r[s][2]};
      D0 = mfma16(Wt[0][s], ob, D0);
      if (j == 3) D1 = mfma16(Wt[1][s], ob, D1);
    }
    {
      const __amdgpu_buffer_rsrc_t rs = __builtin_amdgcn_make_buffer_rsrc(ws + WS_SBOX, 0, NB * SBOX_B, 0x00020000);
      auto send = [&](const v4f& D, int ct) {
        const v4u g = {__float_as_uint(c < 8 ? D[0] : D[2]), tag, __float_as_uint(c < 8 ? D[1] : D[3]), tag};
        __builtin_amdgcn_raw_buffer_store_b128(g, rs, (((c & 7) * 40 + 5 * b + ct) * NH + h) * 128 + t * 32 + (c >> 3) * 16, 0, 16 + (S2NT & 1) * 2);
      };
      send(D0, j);
      if (j == 3) send(D1, 4);
    }
    if (wave == 4) STAMP(4);
    gw = j;
  }
  if (gw < 0) return;
  {
    wait4(&sm.ctl[GDONE]);
    const unsigned tag = sm.ctl[TAG];
    const int cp = 2 * gw + (lane >> 5), prod = lane & 31;
    unsigned xw = sm.xfin[cp];
    float y0 = 0.f, y1 = 0.f;
#pragma unroll
    for (int w = 0; w < 4; ++w) { y0 += sm.gpart[w][2 * cp]; y1 += sm.gpart[w][2 * cp + 1]; }
    const float bb = bflo(sm.xfin[10]);
    y0 = rbf(rbf(y0) + bb); y1 = rbf(rbf(y1) + bb);
    float g0 = rbf(y0 / (1.f + __expf(-y0))), g1 = rbf(y1 / (1.f + __expf(-y1)));
    asm volatile("" : "+v"(g0), "+v"(g1), "+v"(xw));
    const int col = COLS * h + 2 * cp;
    const char* gp = ws + WS_SBOX + (size_t)b * SBOX_B + ((col >> 4) * NH + prod) * 128 + (col & 15) * 8;
    if (S2DELAY) __builtin_amdgcn_s_sleep(S2DELAY);
    v4u g = {0u, 0u, 0u, 0u};
    {
      bool ok = false, full = true;
      unsigned miss = ~0u;
      for (unsigned spins = 0; spins < ((ABL & 64) ? 0 : SPIN_CAP); ++spins) {
        const bool act = !ok && (full || __builtin_popcount(miss & ((1u << prod) - 1u)) < 2);
        if (act) {
          asm volatile("global_load_dwordx4 %0, %1, off " S2POL "\n\ts_waitcnt vmcnt(0)" : "+v"(g) : "v"(gp) : "memory");
          ok = g[1] == tag && g[3] == tag;
        }
        const unsigned long long bm = __ballot(!ok);
        if (!bm) break;
        const unsigned nm = (unsigned)bm | (unsigned)(bm >> 32);
        full = nm != miss;
        miss = nm;
        if (!full) { if (__builtin_popcount(miss) > 4) __builtin_amdgcn_s_sleep(8); else __builtin_amdgcn_s_sleep(1); }
      }
    }
    const float s0 = rbf(half32_sum(__uint_as_float(g[0]))), s1 = rbf(half32_sum(__uint_as_float(g[2])));
    const unsigned res = cvtpk(bflo(xw) + rbf(s0 * g0), bfhi(xw) + rbf(s1 * g1));
    {
      const __amdgpu_buffer_rsrc_t rout = __builtin_amdgcn_make_buffer_rsrc(out, 0, NB * DM * 2, 0x00020000);
      if (prod == 0) __builtin_amdgcn_raw_buffer_store_b32(res, rout, (b * DM + COLS * h + 2 * cp) * 2, 0, OUTPOL);
    }
    if (bid == 0 && tid == 0) __hip_atomic_store(epoch_p, tag, __ATOMIC_RELAXED, __HIP_MEMORY_SCOPE_AGENT);
  }
}

extern "C" void kernel_launch(void* const* d_in, const int* in_sizes, int n_in, void* d_out, int out_size, void* d_ws,
                              size_t ws_size, hipStream_t stream) {
  fprintf(stderr, "PTRS x %p K %p V %p wq %p wo %p bx %p w1 %p b1 %p w2 %p b2 %p out %p ws %p\n", d_in[0], d_in[1], d_in[2], d_in[3], d_in[4], d_in[5], d_in[6], d_in[7], d_in[8], d_in[9], d_out, d_ws);
  { hipPointerAttribute_t at; if (hipPointerGetAttributes(&at, d_in[1]) == hipSuccess) fprintf(stderr, "ATTR type %d dev %d managed %d flags %u\n", (int)at.type, at.device, at.isManaged, at.allocationFlags);
    void* base = 0; size_t sz = 0; if (hipMemGetAddressRange((hipDeviceptr_t*)&base, &sz, (hipDeviceptr_t)d_in[1]) == hipSuccess) fprintf(stderr, "RANGE K base %p size %zu\n", base, sz);
    if (hipMemGetAddressRange((hipDeviceptr_t*)&base, &sz, (hipDeviceptr_t)d_in[2]) == hipSuccess) fprintf(stderr, "RANGE V base %p size %zu\n", base, sz);
    if (hipMemGetAddressRange((hipDeviceptr_t*)&base, &sz, (hipDeviceptr_t)d_ws) == hipSuccess) fprintf(stderr, "RANGE ws base %p size %zu\n", base, sz); (void)hipGetLastError(); }
  fused_decode<<<256, 512, 0, stream>>>((const uint16_t*)d_in[0], (const char*)d_in[1], (const char*)d_in[2],
                                         (const char*)d_in[3], (const char*)d_in[4], (const uint16_t*)d_in[6],
                                         (const uint16_t*)d_in[7], (const uint16_t*)d_in[8], (const uint16_t*)d_in[9],
                                         (uint16_t*)d_out, (char*)d_ws);
}
```

```cpp
#include <hip/hip_runtime.h>
#include <hip/hip_bf16.h>
#include <stdint.h>

#define GAS __attribute__((address_space(1)))
#define LAS __attribute__((address_space(3)))
typedef unsigned v4u __attribute__((ext_vector_type(4)));
typedef unsigned v2u __attribute__((ext_vector_type(2)));
typedef float v4f __attribute__((ext_vector_type(4)));
typedef float v2f __attribute__((ext_vector_type(2)));
typedef __bf16 bf16x8 __attribute__((ext_vector_type(8)));
typedef __bf16 bf16x2 __attribute__((ext_vector_type(2)));

#define DMIN 18
#define DSTEP 2
#define NLEV 6
#define LSH 6
#define TAU 856
#define TOFF 300
#define OUTPOL 0
#define O1NT 0
#define S2NT 0
#define O1DELAY 0
#define S2DELAY 0
#define O1POL "sc1"
#define S2POL "sc1"
#define ABL 0
#define NPRE 3

constexpr int NB = 8, DM = 640, NH = 32, SK = 4096, DH = 128, FF = 1728;
constexpr int COLS = DM / NH;
constexpr float LOG2E = 1.4426950408889634f;
constexpr float SCALE = 0.08838834764831845f;
constexpr unsigned SPIN_CAP = 1u << 20;

constexpr size_t WS_OBOX = 4096, OBOX_B = 512, WS_SBOX = WS_OBOX + 256 * OBOX_B, SBOX_B = 40 * NH * 128;

__device__ __forceinline__ v4f mfma16(v4u a, v4u b, v4f c) {
  return __builtin_amdgcn_mfma_f32_16x16x32_bf16(__builtin_bit_cast(bf16x8, a), __builtin_bit_cast(bf16x8, b), c, 0, 0, 0);
}
__device__ __forceinline__ unsigned cvtpk(float lo, float hi) {
  bf16x2 r = {(__bf16)lo, (__bf16)hi};
  return __builtin_bit_cast(unsigned, r);
}
__device__ __forceinline__ float bflo(unsigned u) { return __builtin_bit_cast(float, u << 16); }
__device__ __forceinline__ float bfhi(unsigned u) { return __builtin_bit_cast(float, u & 0xffff0000u); }
__device__ __forceinline__ float rbf(float x) { return (float)(__bf16)x; }
__device__ __forceinline__ v4u ldnt(const void* p) { return __builtin_nontemporal_load((const GAS v4u*)p); }
__device__ __forceinline__ unsigned ldnt16(const uint16_t* p) { return __builtin_nontemporal_load((const GAS uint16_t*)p); }
template <int D> __device__ __forceinline__ void dma4(unsigned lds, const char* base, unsigned o0, unsigned o1, unsigned o2, unsigned o3) {
  asm volatile("s_mov_b32 m0, %0\n\t"
               "s_waitcnt vmcnt(%6)\n\tglobal_load_lds_dwordx4 %2, %1 nt\n\t"
               "s_waitcnt vmcnt(%6)\n\tglobal_load_lds_dwordx4 %3, %1 offset:1024 nt\n\t"
               "s_waitcnt vmcnt(%6)\n\tglobal_load_lds_dwordx4 %4, %1 offset:2048 nt\n\t"
               "s_waitcnt vmcnt(%6)\n\tglobal_load_lds_dwordx4 %5, %1 offset:3072 nt"
               :: "s"(lds), "s"(base), "v"(o0), "v"(o1), "v"(o2), "v"(o3), "i"(D - 1) : "memory");
}
__device__ __forceinline__ void dma1(unsigned lds, const char* base, unsigned o) {
  asm volatile("s_mov_b32 m0, %0\n\ts_nop 0\n\tglobal_load_lds_dwordx4 %2, %1 nt" :: "s"(lds), "s"(base), "v"(o) : "memory");
}
__device__ __forceinline__ unsigned lds_ld(const unsigned* p) { return __builtin_amdgcn_readfirstlane(*(const volatile LAS unsigned*)p); }
__device__ __forceinline__ void lds_st(unsigned* p, unsigned v) { *(volatile LAS unsigned*)p = v; }

template <int CTRL> __device__ __forceinline__ float dpp(float x) {
  return __builtin_bit_cast(float, __builtin_amdgcn_mov_dpp(__builtin_bit_cast(int, x), CTRL, 0xf, 0xf, true));
}
constexpr int XOR1 = 0xB1, XOR2 = 0x4E, XOR7 = 0x141, XOR15 = 0x140;
__device__ __forceinline__ float swap16_sum(float x) {
  auto s = __builtin_amdgcn_permlane16_swap(__float_as_uint(x), __float_as_uint(x), false, false);
  return __uint_as_float(s[0]) + __uint_as_float(s[1]);
}
__device__ __forceinline__ float swap32_sum(float x) {
  auto s = __builtin_amdgcn_permlane32_swap(__float_as_uint(x), __float_as_uint(x), false, false);
  return __uint_as_float(s[0]) + __uint_as_float(s[1]);
}
__device__ __forceinline__ float swap16_max(float x) {
  auto s = __builtin_amdgcn_permlane16_swap(__float_as_uint(x), __float_as_uint(x), false, false);
  return fmaxf(__uint_as_float(s[0]), __uint_as_float(s[1]));
}
__device__ __forceinline__ float swap32_max(float x) {
  auto s = __builtin_amdgcn_permlane32_swap(__float_as_uint(x), __float_as_uint(x), false, false);
  return fmaxf(__uint_as_float(s[0]), __uint_as_float(s[1]));
}
__device__ __forceinline__ float row16_sum(float x) {
  x += dpp<XOR1>(x); x += dpp<XOR2>(x); x += dpp<XOR7>(x); x += dpp<XOR15>(x); return x;
}
__device__ __forceinline__ float half32_sum(float x) { return swap16_sum(row16_sum(x)); }
__device__ __forceinline__ float wave_sum(float x) { return swap32_sum(half32_sum(x)); }

__device__ __forceinline__ float wave_max(float x) {
  x = fmaxf(x, dpp<XOR1>(x)); x = fmaxf(x, dpp<XOR2>(x)); x = fmaxf(x, dpp<XOR7>(x)); x = fmaxf(x, dpp<XOR15>(x));
  return swap32_max(swap16_max(x));
}
__device__ __forceinline__ void wg_barrier() {
  asm volatile("s_waitcnt lgkmcnt(0)" ::: "memory");
  __builtin_amdgcn_s_barrier();
  asm volatile("" ::: "memory");
}

__device__ __forceinline__ float ln_apply(float x, float m, float rden) { return rbf(rbf(x - m) * rden); }
template <int N, bool HALF>
__device__ __forceinline__ void ln_stats(const float (&v)[N], bool tail, float& m, float& den) {
  float s = 0.f, s2 = 0.f;
#pragma unroll
  for (int i = 0; i < N - 8; ++i) s += v[i];
#pragma unroll
  for (int i = N - 8; i < N; ++i) s2 += v[i];
  s += tail ? s2 : 0.f;
  s = HALF ? half32_sum(s) : wave_sum(s);
  const float mean = s * (1.0f / DM);
  float q = 0.f, q2 = 0.f;
#pragma unroll
  for (int i = 0; i < N - 8; ++i) { const float d = v[i] - mean; q = fmaf(d, d, q); }
#pragma unroll
  for (int i = N - 8; i < N; ++i) { const float d = v[i] - mean; q2 = fmaf(d, d, q2); }
  q += tail ? q2 : 0.f;
  q = HALF ? half32_sum(q) : wave_sum(q);
  const float var = rbf(q * (1.0f / DM));
  m = rbf(mean);
  den = __builtin_amdgcn_rcpf(rbf(__builtin_sqrtf(rbf(var + 1e-5f))));
}
__device__ __forceinline__ float rdlane(float x, int l) { return __builtin_bit_cast(float, __builtin_amdgcn_readlane(__builtin_bit_cast(int, x), l)); }

#define STAMP(k) do {} while (0)
constexpr int NSLOT = 8, TILE_B = 16384, NKT = SK / 64, NTILE = 2 * NKT, NCONS = 3;
constexpr int LASTC = (NTILE - 1) % NCONS;
struct Smem {
  unsigned char ring[NSLOT * TILE_B];
  uint16_t wblk[DH * 80];
  uint16_t sc[SK];
  float qpart[4][DH];
  unsigned qbf[NCONS][64];
  float ml[NCONS][2];
  float pad[2];
  float gpart[4][32];
  unsigned xfin[16];
  unsigned ctl[32];
};
constexpr int QFLAG = 0, WLAND = 4, FREED = 8, LANDED = 12, TAG = 15, KDONE = 16, VDONE = 20, GDONE = 24;

__device__ __forceinline__ void wait4(const unsigned* p) {
  for (;;) {
    const v4u f = *(const volatile LAS v4u*)p;
    if (__builtin_amdgcn_readfirstlane(f[0] & f[1] & f[2] & f[3])) break;
    __builtin_amdgcn_s_sleep(1);
  }
  asm volatile("" ::: "memory");
}

template <int D, typename F>
__device__ __forceinline__ void tile(int n, unsigned slot, const char* src, const unsigned (&of)[4], F& publish) {
  constexpr int PH = ((D + 3) / 4 + 1) & 1;
  dma4<D>(slot, src, of[0], of[1], of[2], of[3]);
  if (PH == 0) publish((16 * n + 4 - D) >> 3);
  dma4<D>(slot + 4096, src + 4096, of[0], of[1], of[2], of[3]);
  if (PH == 1) publish((16 * n + 8 - D) >> 3);
  dma4<D>(slot + 8192, src + 8192, of[0], of[1], of[2], of[3]);
  if (PH == 0) publish((16 * n + 12 - D) >> 3);
  dma4<D>(slot + 12288, src + 12288, of[0], of[1], of[2], of[3]);
  if (PH == 1) publish((16 * n + 16 - D) >> 3);
}

__global__ void __launch_bounds__(512, 2)
fused_decode(const uint16_t* __restrict__ x, const char* __restrict__ kc, const char* __restrict__ vc,
             const char* __restrict__ wq, const char* __restrict__ wo, const uint16_t* __restrict__ w1,
             const uint16_t* __restrict__ b1, const uint16_t* __restrict__ w2, const uint16_t* __restrict__ b2,
             uint16_t* __restrict__ out, char* ws) {
  __shared__ __attribute__((aligned(16))) Smem sm;
  const int tid = threadIdx.x, lane = tid & 63, wave = __builtin_amdgcn_readfirstlane(tid >> 6);
  const int bid = blockIdx.x;
  const int h = (bid & 7) * 4 + ((bid >> 3) & 3), b = bid >> 5;
  const int t = lane >> 4, c = lane & 15;
  v4u dmask;
#pragma unroll
  for (int i = 0; i < 4; ++i) dmask[i] = ((c & 7) >> 1) == i ? ((c & 1) ? 0xffff0000u : 0x0000ffffu) : 0u;
  auto diag = [&](unsigned dup) { v4u a; a[0] = dup & dmask[0]; a[1] = dup & dmask[1]; a[2] = dup & dmask[2]; a[3] = dup & dmask[3]; return a; };
  GAS unsigned* epoch_p = (GAS unsigned*)ws;
  const unsigned tk0 = (unsigned)__builtin_amdgcn_s_memrealtime();
  int gw;

  if (wave == 0) {
    STAMP(0);
    __builtin_amdgcn_s_setprio(3);
    const size_t hb = ((size_t)(b * NH + h)) * (SK * DH * 2);
    const char* src = kc + hb;
    unsigned of[4];
#pragma unroll
    for (int i = 0; i < 4; ++i) of[i] = t * 256 + ((c ^ (4 * i + t)) * 16);
    const unsigned ring0 = (unsigned)(size_t)(LAS unsigned char*)sm.ring;
    unsigned slot = ring0;
    int pub = 0;
    auto publish = [&](int v) { if (v > pub) { pub = v; lds_st(&sm.ctl[LANDED], (unsigned)v); } };
    dma4<63>(slot, src, of[0], of[1], of[2], of[3]);
    dma4<63>(slot + 4096, src + 4096, of[0], of[1], of[2], of[3]);
    wg_barrier();
    dma4<63>(slot + 8192, src + 8192, of[0], of[1], of[2], of[3]);
    dma4<63>(slot + 12288, src + 12288, of[0], of[1], of[2], of[3]);
    const unsigned tstart = (unsigned)__builtin_amdgcn_s_memrealtime();
    int cons = (NCONS - NSLOT % NCONS) % NCONS;
    for (int n = 1; n < NTILE; ++n) {
      src += TILE_B;
      if (n == NKT) { src = vc + hb; of[0] = of[1] = of[2] = of[3] = lane * 16; }
      slot += TILE_B; if (slot == ring0 + NSLOT * TILE_B) slot = ring0;
      cons = cons == NCONS - 1 ? 0 : cons + 1;
      if (n >= NSLOT) {
        const unsigned need = n - NSLOT + 1;
        if (lds_ld(&sm.ctl[FREED + cons]) < need) {
          asm volatile("s_waitcnt vmcnt(0)" ::: "memory");
          publish(2 * n);
          while (lds_ld(&sm.ctl[FREED + cons]) < need) __builtin_amdgcn_s_sleep(1);
        }
      }
      const int lev = ((int)((unsigned)__builtin_amdgcn_s_memrealtime() - tstart) - TOFF - ((n * TAU) >> 4)) >> LSH;
      if (lev <= 0) tile<DMIN>(n, slot, src, of, publish);
      else if (lev == 1 || NLEV == 2) tile<DMIN + DSTEP>(n, slot, src, of, publish);
      else if (lev == 2 || NLEV == 3) tile<DMIN + 2 * DSTEP>(n, slot, src, of, publish);
      else if (lev == 3 || NLEV == 4) tile<DMIN + 3 * DSTEP>(n, slot, src, of, publish);
      else if (lev == 4 || NLEV == 5) tile<DMIN + 4 * DSTEP>(n, slot, src, of, publish);
      else tile<DMIN + 5 * DSTEP>(n, slot, src, of, publish);
    }
    asm volatile("s_waitcnt vmcnt(24)" ::: "memory");
    publish(2 * NTILE - 3);
    asm volatile("s_waitcnt vmcnt(16)" ::: "memory");
    publish(2 * NTILE - 2);
    asm volatile("s_waitcnt vmcnt(8)" ::: "memory");
    publish(2 * NTILE - 1);
    asm volatile("s_waitcnt vmcnt(0)" ::: "memory");
    publish(2 * NTILE);
    STAMP(6);
    __builtin_amdgcn_s_setprio(0);
    gw = 4;
  } else if (wave < 4) {
    const int ci = wave - 1;
    if (wave == 1 && lane < 32) sm.ctl[lane] = (lane == KDONE + 3 || lane == VDONE + 3 || lane == VDONE + LASTC) ? 1u : 0u;
    wg_barrier();
    unsigned kof[4];
#pragma unroll
    for (int s = 0; s < 4; ++s) kof[s] = c * 256 + (((4 * s + t) ^ c) * 16);
    int slot = ci, n = ci;
    auto fetchK = [&](v4u (&Kr)[16]) {
      const unsigned char* tb = sm.ring + slot * TILE_B;
      while (lds_ld(&sm.ctl[LANDED]) < (unsigned)(2 * n + 2)) __builtin_amdgcn_s_sleep(1);
      asm volatile("" ::: "memory");
#pragma unroll
      for (int u = 0; u < 4; ++u)
#pragma unroll
        for (int s = 0; s < 4; ++s) Kr[4 * u + s] = *(const v4u*)(tb + u * 4096 + kof[s]);
      asm volatile("s_waitcnt lgkmcnt(0)" ::: "memory");
      lds_st(&sm.ctl[FREED + ci], n + 1);
#pragma unroll
      for (int i = 0; i < 16; ++i) asm volatile("" : "+v"(Kr[i]));
      slot += NCONS; slot = slot >= NSLOT ? slot - NSLOT : slot;
      n += NCONS;
    };
    v4u Kp[NPRE][16];
#pragma unroll
    for (int k = 0; k < NPRE; ++k) fetchK(Kp[k]);
    wait4(&sm.ctl[QFLAG]);
    if (wave == 1) STAMP(1);
    v4u qf[4];
    {
      float q0 = 0.f, q1 = 0.f;
#pragma unroll
      for (int j = 0; j < 4; ++j) { const v2f p = *(const v2f*)&sm.qpart[j][2 * lane]; q0 += p[0]; q1 += p[1]; }
      sm.qbf[ci][lane] = cvtpk(q0, q1);
      asm volatile("s_waitcnt lgkmcnt(0)" ::: "memory");
#pragma unroll
      for (int s = 0; s < 4; ++s) qf[s] = *(const v4u*)&sm.qbf[ci][16 * s + 4 * t];
    }
    float m = -INFINITY, l = 0.f;
    auto scoreK = [&](const v4u (&Kr)[16], int tile) {
      v4f S[4];
#pragma unroll
      for (int u = 0; u < 4; ++u) S[u] = v4f{0.f, 0.f, 0.f, 0.f};
#pragma unroll
      for (int s = 0; s < 4; ++s)
#pragma unroll
        for (int u = 0; u < 4; ++u) S[u] = mfma16(qf[s], Kr[4 * u + s], S[u]);
      const float sr = t == 0 ? S[0][0] : t == 1 ? S[1][0] : t == 2 ? S[2][0] : S[3][0];
      const unsigned sb = cvtpk(rbf(sr) * SCALE, 0.f);
      sm.sc[64 * tile + lane] = (uint16_t)sb;
      const float sv = bflo(sb) * LOG2E;
      const float mn = fmaxf(m, sv);
      l = fmaf(l, __builtin_amdgcn_exp2f(m - mn), __builtin_amdgcn_exp2f(sv - mn));
      m = mn;
    };
#pragma unroll
    for (int k = 0; k < NPRE; ++k) scoreK(Kp[k], ci + NCONS * k);
    while (n < NKT) {
      v4u Kr[16];
      fetchK(Kr);
      scoreK(Kr, n - NCONS);
    }
    {
      const float mw = wave_max(m);
      const float lw = wave_sum(l * __builtin_amdgcn_exp2f(m - mw));
      if (lane == 0) { sm.ml[ci][0] = mw; sm.ml[ci][1] = lw; }
      asm volatile("s_waitcnt lgkmcnt(0)" ::: "memory");
      lds_st(&sm.ctl[KDONE + ci], 1u);
      wait4(&sm.ctl[KDONE]);
    }
    float M = -INFINITY, L = 0.f;
#pragma unroll
    for (int w = 0; w < NCONS; ++w) M = fmaxf(M, sm.ml[w][0]);
#pragma unroll
    for (int w = 0; w < NCONS; ++w) L = fmaf(sm.ml[w][1], __builtin_amdgcn_exp2f(sm.ml[w][0] - M), L);
    const float invL = 1.0f / L;
    const unsigned tag = sm.ctl[TAG];
    if (wave == 1) STAMP(13);
    v4f O0 = {0.f, 0.f, 0.f, 0.f}, O1 = {0.f, 0.f, 0.f, 0.f};
    const unsigned vof = t * 1024 + c * 16;
    for (; n < NTILE; n += NCONS) {
      const unsigned char* tb = sm.ring + slot * TILE_B + vof;
      unsigned pd[16];
#pragma unroll
      for (int qd = 0; qd < 4; ++qd) {
        const v2u sv = *(const v2u*)&sm.sc[64 * (n - NKT) + 16 * qd + 4 * t];
#pragma unroll
        for (int i = 0; i < 2; ++i) {
          const float p0 = __builtin_amdgcn_exp2f(fmaf(bflo(sv[i]), LOG2E, -M)) * invL, p1 = __builtin_amdgcn_exp2f(fmaf(bfhi(sv[i]), LOG2E, -M)) * invL;
          pd[4 * qd + 2 * i] = cvtpk(p0, p0); pd[4 * qd + 2 * i + 1] = cvtpk(p1, p1);
        }
      }
#pragma unroll
      for (int hf = 0; hf < 2; ++hf) {
        v4u pa[8];
#pragma unroll
        for (int i = 0; i < 8; ++i) pa[i] = diag(pd[8 * hf + i]);
#pragma unroll
        for (int i = 0; i < 8; ++i) asm volatile("" : "+v"(pa[i]));
        if (hf == 1 && n == NTILE - 1) {
          wait4(&sm.ctl[VDONE]);
#pragma unroll
          for (int w = 0; w < NCONS; ++w)
            if (w != LASTC) O1 += *(const v4f*)&sm.qpart[w][8 * c + 4 * (t & 1)];
          asm volatile("" : "+v"(O1));
        }
        while (lds_ld(&sm.ctl[LANDED]) < (unsigned)(2 * n + 1 + hf)) {}
        asm volatile("" ::: "memory");
        v4u Vr[8];
#pragma unroll
        for (int i = 0; i < 4; ++i) { Vr[i] = *(const v4u*)(tb + hf * 8192 + i * 256); Vr[4 + i] = *(const v4u*)(tb + hf * 8192 + 4096 + i * 256); }
#pragma unroll
        for (int i = 0; i < 4; ++i) { O0 = mfma16(pa[i], Vr[i], O0); O1 = mfma16(pa[4 + i], Vr[4 + i], O1); }
      }
      asm volatile("s_waitcnt lgkmcnt(0)" ::: "memory");
      lds_st(&sm.ctl[FREED + ci], n + 1);
      slot += NCONS; slot = slot >= NSLOT ? slot - NSLOT : slot;
    }
    O0 += O1;
    if (ci == LASTC) {
      const __amdgpu_buffer_rsrc_t ro = __builtin_amdgcn_make_buffer_rsrc(ws + WS_OBOX + (size_t)(h * NB + b) * OBOX_B, 0, OBOX_B, 0x00020000);
      const v4u g = {cvtpk(O0[0], O0[1]), tag, cvtpk(O0[2], O0[3]), tag};
      if (lane < 32) __builtin_amdgcn_raw_buffer_store_b128(g, ro, (2 * c + t) * 16, 0, 16 + (O1NT & 1) * 2);
      STAMP(2);
    } else {
      if (lane < 32) *(v4f*)&sm.qpart[ci][8 * c + 4 * t] = O0;
      asm volatile("s_waitcnt lgkmcnt(0)" ::: "memory");
      lds_st(&sm.ctl[VDONE + ci], 1u);
    }
    gw = -1;
  } else {
    const int j = wave - 4;
    wg_barrier();
    const uint16_t* xr = x + b * DM;
    const v4u xa = ldnt(xr + 8 * lane), xb = ldnt(xr + 512 + 8 * c);
    float m1, den1, xv[16];
    unsigned tag;
    {
      v4u xq[5];
#pragma unroll
      for (int n = 0; n < 5; ++n) xq[n] = ldnt(xr + 160 * j + 40 * t + 8 * n);
      unsigned ep = 0;
      if (j == 0) ep = __hip_atomic_load(epoch_p, __ATOMIC_RELAXED, __HIP_MEMORY_SCOPE_AGENT);
      v4u W[40];
      const char* p = wq + (size_t)(160 * j + 40 * t) * (NH * DH * 2) + h * (DH * 2) + c * 16;
#pragma unroll
      for (int i = 0; i < 40; ++i) W[i] = (ABL & 1) ? dmask : ldnt(p + (size_t)i * (NH * DH * 2));
      __builtin_amdgcn_sched_barrier(0);
#pragma unroll
      for (int i = 0; i < 4; ++i) { xv[2 * i] = bflo(xa[i]); xv[2 * i + 1] = bfhi(xa[i]); xv[8 + 2 * i] = bflo(xb[i]); xv[9 + 2 * i] = bfhi(xb[i]); }
      ln_stats<16, false>(xv, lane < 16, m1, den1);
      v4f Q0 = {0.f, 0.f, 0.f, 0.f}, Q1 = {0.f, 0.f, 0.f, 0.f};
#pragma unroll
      for (int i = 0; i < 20; ++i) {
        const unsigned w = xq[i >> 2][i & 3];
        const float e0 = rbf(bflo(w) - m1) * den1, e1 = rbf(bfhi(w) - m1) * den1;
        Q0 = mfma16(diag(cvtpk(e0, e0)), W[2 * i], Q0);
        Q1 = mfma16(diag(cvtpk(e1, e1)), W[2 * i + 1], Q1);
      }
      Q0 += Q1;
      if (lane < 32) *(v4f*)&sm.qpart[j][8 * c + 4 * t] = Q0;
      if (j == 0) lds_st(&sm.ctl[TAG], ep + 1u);
      asm volatile("s_waitcnt lgkmcnt(0)" ::: "memory");
      lds_st(&sm.ctl[QFLAG + j], 1u);
      if (wave == 4) STAMP(8);
    }
    if (!(ABL & 8)) {
      const char* wb = wo + (size_t)(DH * h + 32 * j) * (DM * 2) + b * 160;
      const unsigned w0 = (unsigned)(size_t)(LAS uint16_t*)sm.wblk + j * 5120;
#pragma unroll
      for (int p = 0; p < 5; ++p) {
        const unsigned id = 64 * p + lane, row = id / 10;
        dma1(w0 + p * 1024, wb, row * (DM * 2) + (id - 10 * row) * 16);
      }
    }
    if (!(ABL & 4)) {
      unsigned r1[7], r2[7], r3[7];
#pragma unroll
      for (int i = 0; i < 7; ++i) {
        const int f = 256 * i + 64 * (i == 6 && j == 3 ? 0 : j) + lane;
        r1[i] = ldnt16(w1 + f); r2[i] = ldnt16(b1 + f); r3[i] = ldnt16(w2 + f);
      }
      const unsigned xcol = ldnt16(xr + COLS * h + (lane < COLS ? lane : 0));
      const unsigned bb = ldnt16(b2);
      r3[6] = j == 3 ? 0u : r3[6];
      float gw1[7], gb1[7], gw2[7];
#pragma unroll
      for (int i = 0; i < 7; ++i) { gw1[i] = bflo(r1[i]); gb1[i] = bflo(r2[i]); gw2[i] = bflo(r3[i]); }
      float m2, den2;
#pragma unroll
      for (int i = 0; i < 16; ++i) xv[i] = ln_apply(xv[i], m1, den1);
      ln_stats<16, false>(xv, lane < 16, m2, den2);
      const float hv = ln_apply(ln_apply(bflo(xcol), m1, den1), m2, den2);
      float gsum = 0.f;
#pragma unroll 2
      for (int col = 0; col < COLS; ++col) {
        const float hc = rdlane(hv, col);
        float acc = 0.f;
#pragma unroll
        for (int i = 0; i < 7; ++i) {
          const float u = rbf(rbf(hc * gw1[i]) + gb1[i]);
          const float z = u * fmaf(u * u, 0.044715f * 0.7978845608028654f * 2.f * LOG2E, 0.7978845608028654f * 2.f * LOG2E);
          const float ge = rbf(u * __builtin_amdgcn_rcpf(1.f + __builtin_amdgcn_exp2f(-z)));
          acc = fmaf(ge, gw2[i], acc);
        }
        acc = wave_sum(acc);
        gsum = lane == col ? acc : gsum;
      }
      if (lane < COLS) sm.gpart[j][lane] = gsum;
      if (j == 0) {
        if (lane < COLS) ((uint16_t*)sm.xfin)[lane] = (uint16_t)xcol;
        if (lane == 0) sm.xfin[10] = bb;
      }
    }
    asm volatile("s_waitcnt vmcnt(0) lgkmcnt(0)" ::: "memory");
    lds_st(&sm.ctl[GDONE + j], 1u);
    wait4(&sm.ctl[GDONE]);
    tag = sm.ctl[TAG];
    v4u Wt[2][4];
    {
      const uint16_t* wl = sm.wblk + 8 * t * 80 + c;
#pragma unroll
      for (int e = 0; e < 2; ++e)
#pragma unroll
        for (int s = 0; s < 4; ++s)
#pragma unroll
          for (int i = 0; i < 4; ++i) {
            const uint16_t* q = wl + (32 * s + 2 * i) * 80 + (e ? 64 : 16 * j);
            Wt[e][s][i] = (unsigned)q[0] | ((unsigned)q[80] << 16);
          }
#pragma unroll
      for (int s = 0; s < 4; ++s) asm volatile("" : "+v"(Wt[0][s]), "+v"(Wt[1][s]));
    }
    if (wave == 4) STAMP(3);
    while (lds_ld(&sm.ctl[LANDED]) < (unsigned)(2 * NTILE)) __builtin_amdgcn_s_sleep(2);
    if (O1DELAY) __builtin_amdgcn_s_sleep(O1DELAY);
    const char* gp = ws + WS_OBOX + (size_t)(h * NB + (c & 7)) * OBOX_B + (2 * t + (c >> 3)) * 16;
    v4u r[4];
#pragma unroll
    for (int s = 0; s < 4; ++s) r[s] = v4u{0u, 0u, 0u, 0u};
    {
      bool ok = false;
      for (unsigned spins = 0; spins < ((ABL & 64) ? 0 : SPIN_CAP); ++spins) {
        if (!ok) {
          asm volatile("global_load_dwordx4 %0, %4, off " O1POL "\n\tglobal_load_dwordx4 %1, %4, off offset:128 " O1POL "\n\t"
                       "global_load_dwordx4 %2, %4, off offset:256 " O1POL "\n\tglobal_load_dwordx4 %3, %4, off offset:384 " O1POL "\n\ts_waitcnt vmcnt(0)"
                       : "+v"(r[0]), "+v"(r[1]), "+v"(r[2]), "+v"(r[3]) : "v"(gp) : "memory");
          ok = r[0][1] == tag && r[0][3] == tag && r[1][1] == tag && r[1][3] == tag && r[2][1] == tag && r[2][3] == tag && r[3][1] == tag && r[3][3] == tag;
        }
        const unsigned long long bm = __ballot(!ok);
        if (!bm) break;
        if (__builtin_popcountll(bm) > 16) __builtin_amdgcn_s_sleep(8); else __builtin_amdgcn_s_sleep(1);
      }
    }
    if (wave == 4) STAMP(7);
    v4f D0 = {0.f, 0.f, 0.f, 0.f}, D1 = {0.f, 0.f, 0.f, 0.f};
#pragma unroll
    for (int s = 0; s < 4; ++s) {
      const unsigned y0 = __builtin_amdgcn_mov_dpp(r[s][0], 0x128, 0xf, 0xf, true), y1 = __builtin_amdgcn_mov_dpp(r[s][2], 0x128, 0xf, 0xf, true);
      const v4u ob = c < 8 ? v4u{r[s][0], r[s][2], y0, y1} : v4u{y0, y1, r[s][0], r[s][2]};
      D0 = mfma16(Wt[0][s], ob, D0);
      if (j == 3) D1 = mfma16(Wt[1][s], ob, D1);
    }
    {
      const __amdgpu_buffer_rsrc_t rs = __builtin_amdgcn_make_buffer_rsrc(ws + WS_SBOX, 0, NB * SBOX_B, 0x00020000);
      auto send = [&](const v4f& D, int ct) {
        const v4u g = {__float_as_uint(c < 8 ? D[0] : D[2]), tag, __float_as_uint(c < 8 ? D[1] : D[3]), tag};
        __builtin_amdgcn_raw_buffer_store_b128(g, rs, (((c & 7) * 40 + 5 * b + ct) * NH + h) * 128 + t * 32 + (c >> 3) * 16, 0, 16 + (S2NT & 1) * 2);
      };
      send(D0, j);
      if (j == 3) send(D1, 4);
    }
    if (wave == 4) STAMP(4);
    gw = j;
  }
  if (gw < 0) return;
  {
    wait4(&sm.ctl[GDONE]);
    const unsigned tag = sm.ctl[TAG];
    const int cp = 2 * gw + (lane >> 5), prod = lane & 31;
    unsigned xw = sm.xfin[cp];
    float y0 = 0.f, y1 = 0.f;
#pragma unroll
    for (int w = 0; w < 4; ++w) { y0 += sm.gpart[w][2 * cp]; y1 += sm.gpart[w][2 * cp + 1]; }
    const float bb = bflo(sm.xfin[10]);
    y0 = rbf(rbf(y0) + bb); y1 = rbf(rbf(y1) + bb);
    float g0 = rbf(y0 / (1.f + __expf(-y0))), g1 = rbf(y1 / (1.f + __expf(-y1)));
    asm volatile("" : "+v"(g0), "+v"(g1), "+v"(xw));
    const int col = COLS * h + 2 * cp;
    const char* gp = ws + WS_SBOX + (size_t)b * SBOX_B + ((col >> 4) * NH + prod) * 128 + (col & 15) * 8;
    if (S2DELAY) __builtin_amdgcn_s_sleep(S2DELAY);
    v4u g = {0u, 0u, 0u, 0u};
    {
      bool ok = false, full = true;
      unsigned miss = ~0u;
      for (unsigned spins = 0; spins < ((ABL & 64) ? 0 : SPIN_CAP); ++spins) {
        const bool act = !ok && (full || __builtin_popcount(miss & ((1u << prod) - 1u)) < 2);
        if (act) {
          asm volatile("global_load_dwordx4 %0, %1, off " S2POL "\n\ts_waitcnt vmcnt(0)" : "+v"(g) : "v"(gp) : "memory");
          ok = g[1] == tag && g[3] == tag;
        }
        const unsigned long long bm = __ballot(!ok);
        if (!bm) break;
        const unsigned nm = (unsigned)bm | (unsigned)(bm >> 32);
        full = nm != miss;
        miss = nm;
        if (!full) { if (__builtin_popcount(miss) > 4) __builtin_amdgcn_s_sleep(8); else __builtin_amdgcn_s_sleep(1); }
      }
    }
    while ((int)((unsigned)__builtin_amdgcn_s_memrealtime() - tk0) < 15000) __builtin_amdgcn_s_sleep(1);
    const float s0 = rbf(half32_sum(__uint_as_float(g[0]))), s1 = rbf(half32_sum(__uint_as_float(g[2])));
    const unsigned res = cvtpk(bflo(xw) + rbf(s0 * g0), bfhi(xw) + rbf(s1 * g1));
    {
      const __amdgpu_buffer_rsrc_t rout = __builtin_amdgcn_make_buffer_rsrc(out, 0, NB * DM * 2, 0x00020000);
      if (prod == 0) __builtin_amdgcn_raw_buffer_store_b32(res, rout, (b * DM + COLS * h + 2 * cp) * 2, 0, OUTPOL);
    }
    if (bid == 0 && tid == 0) __hip_atomic_store(epoch_p, tag, __ATOMIC_RELAXED, __HIP_MEMORY_SCOPE_AGENT);
  }
}

extern "C" void kernel_launch(void* const* d_in, const int* in_sizes, int n_in, void* d_out, int out_size, void* d_ws,
                              size_t ws_size, hipStream_t stream) {
  fused_decode<<<256, 512, 0, stream>>>((const uint16_t*)d_in[0], (const char*)d_in[1], (const char*)d_in[2],
                                         (const char*)d_in[3], (const char*)d_in[4], (const uint16_t*)d_in[6],
                                         (const uint16_t*)d_in[7], (const uint16_t*)d_in[8], (const uint16_t*)d_in[9],
                                         (uint16_t*)d_out, (char*)d_ws);
}
```

```cpp
#include <hip/hip_runtime.h>
#include <hip/hip_bf16.h>
#include <stdint.h>

#define GAS __attribute__((address_space(1)))
#define LAS __attribute__((address_space(3)))
typedef unsigned v4u __attribute__((ext_vector_type(4)));
typedef unsigned v2u __attribute__((ext_vector_type(2)));
typedef float v4f __attribute__((ext_vector_type(4)));
typedef float v2f __attribute__((ext_vector_type(2)));
typedef __bf16 bf16x8 __attribute__((ext_vector_type(8)));
typedef __bf16 bf16x2 __attribute__((ext_vector_type(2)));

#define EMU_SCORE 1

constexpr int NB = 8, DM = 640, NH = 32, SK = 4096, DH = 128, FF = 1728;
constexpr int COLS = DM / NH;
constexpr float LOG2E = 1.4426950408889634f;
constexpr float SCALE = 0.08838834764831845f;
constexpr unsigned SPIN_CAP = 1u << 20;

constexpr size_t WS_GRAN = 4096;

__device__ __forceinline__ v4f mfma16(v4u a, v4u b, v4f c) {
  return __builtin_amdgcn_mfma_f32_16x16x32_bf16(__builtin_bit_cast(bf16x8, a), __builtin_bit_cast(bf16x8, b), c, 0, 0, 0);
}
__device__ __forceinline__ unsigned cvtpk(float lo, float hi) {
  bf16x2 r = {(__bf16)lo, (__bf16)hi};
  return __builtin_bit_cast(unsigned, r);
}
__device__ __forceinline__ float bflo(unsigned u) { return __builtin_bit_cast(float, u << 16); }
__device__ __forceinline__ float bfhi(unsigned u) { return __builtin_bit_cast(float, u & 0xffff0000u); }
__device__ __forceinline__ float rbf(float x) { return (float)(__bf16)x; }
__device__ __forceinline__ v4u ldnt(const void* p) { return __builtin_nontemporal_load((const GAS v4u*)p); }
__device__ __forceinline__ v4u ldg(const void* p) { return *(const GAS v4u*)p; }

template <int CTRL> __device__ __forceinline__ float dpp(float x) {
  return __builtin_bit_cast(float, __builtin_amdgcn_mov_dpp(__builtin_bit_cast(int, x), CTRL, 0xf, 0xf, true));
}
constexpr int XOR1 = 0xB1, XOR2 = 0x4E, XOR7 = 0x141, XOR15 = 0x140;
__device__ __forceinline__ float swap16_sum(float x) {
  auto s = __builtin_amdgcn_permlane16_swap(__float_as_uint(x), __float_as_uint(x), false, false);
  return __uint_as_float(s[0]) + __uint_as_float(s[1]);
}
__device__ __forceinline__ float swap32_sum(float x) {
  auto s = __builtin_amdgcn_permlane32_swap(__float_as_uint(x), __float_as_uint(x), false, false);
  return __uint_as_float(s[0]) + __uint_as_float(s[1]);
}
__device__ __forceinline__ float swap16_max(float x) {
  auto s = __builtin_amdgcn_permlane16_swap(__float_as_uint(x), __float_as_uint(x), false, false);
  return fmaxf(__uint_as_float(s[0]), __uint_as_float(s[1]));
}
__device__ __forceinline__ float swap32_max(float x) {
  auto s = __builtin_amdgcn_permlane32_swap(__float_as_uint(x), __float_as_uint(x), false, false);
  return fmaxf(__uint_as_float(s[0]), __uint_as_float(s[1]));
}
__device__ __forceinline__ float row16_sum(float x) {
  x += dpp<XOR1>(x); x += dpp<XOR2>(x); x += dpp<XOR7>(x); x += dpp<XOR15>(x); return x;
}
__device__ __forceinline__ float half32_sum(float x) { return swap16_sum(row16_sum(x)); }
__device__ __forceinline__ float wave_sum(float x) { return swap32_sum(half32_sum(x)); }

__device__ __forceinline__ void wg_barrier() {
  asm volatile("s_waitcnt lgkmcnt(0)" ::: "memory");
  __builtin_amdgcn_s_barrier();
  asm volatile("" ::: "memory");
}

__device__ __forceinline__ void ln_stats(const float (&v)[16], bool has2, float& m, float& den) {
  float s = 0.f;
#pragma unroll
  for (int i = 0; i < 16; ++i) s += v[i];
  s = wave_sum(s);
  const float mean = s * (1.0f / DM);
  float q = 0.f;
#pragma unroll
  for (int i = 0; i < 8; ++i) { const float d = v[i] - mean; q = fmaf(d, d, q); }
  float q2 = 0.f;
#pragma unroll
  for (int i = 8; i < 16; ++i) { const float d = v[i] - mean; q2 = fmaf(d, d, q2); }
  q += has2 ? q2 : 0.f;
  q = wave_sum(q);
  const float var = rbf(q * (1.0f / DM));
  m = rbf(mean);
  den = rbf(__builtin_sqrtf(rbf(var + 1e-5f)));
}
__device__ __forceinline__ float ln_apply(float x, float m, float den) { return rbf(rbf(x - m) / den); }

#define STAMP(k) do {} while (0)
struct Smem {
  float qpart[4][DH];
  unsigned qbf[4][64];
  float comb[4][DH + 4];
  float spart[4][DM];
  float gpart[4][32];
  unsigned tag[4];
};

__global__ void __launch_bounds__(512, 2)
fused_decode(const uint16_t* __restrict__ x, const char* __restrict__ kc, const char* __restrict__ vc,
             const char* __restrict__ wq, const char* __restrict__ wo, const uint16_t* __restrict__ w1,
             const uint16_t* __restrict__ b1, const uint16_t* __restrict__ w2, const uint16_t* __restrict__ b2,
             uint16_t* __restrict__ out, char* ws) {
  __shared__ __attribute__((aligned(16))) Smem sm;
  const int tid = threadIdx.x, lane = tid & 63, wave = __builtin_amdgcn_readfirstlane(tid >> 6);
  const int bid = blockIdx.x;
  const int h = (bid & 7) * 4 + ((bid >> 3) & 3), b = bid >> 5;
  const int t = lane >> 4, c = lane & 15;
  v4u dmask;
#pragma unroll
  for (int i = 0; i < 4; ++i) dmask[i] = (c >> 1) == i ? ((c & 1) ? 0xffff0000u : 0x0000ffffu) : 0u;
  auto diag = [&](unsigned dup) { v4u a; a[0] = dup & dmask[0]; a[1] = dup & dmask[1]; a[2] = dup & dmask[2]; a[3] = dup & dmask[3]; return a; };

  const unsigned tk0 = (unsigned)__builtin_amdgcn_s_memrealtime();
  GAS unsigned* epoch_p = (GAS unsigned*)(ws + b * 256);

  if (wave == 0) STAMP(0);
  if (wave < 4) {
    constexpr int NSTEP = SK / (4 * 32);
    const size_t hb = ((size_t)(b * NH + h)) * (SK * DH * 2);
    const char* kb = kc + hb + wave * 8192 + (4 * (c & 3) + (c >> 2)) * 256 + t * 16;
    const char* vb = vc + hb + wave * 8192 + lane * 16;
    v4u Kr[2][8], Vr[2][8];
    auto issueK = [&](int slot, int st) {
      const char* p = kb + (size_t)st * 32768;
#pragma unroll
      for (int u = 0; u < 2; ++u)
#pragma unroll
        for (int s = 0; s < 4; ++s) Kr[slot][u * 4 + s] = ldnt(p + u * 4096 + s * 64);
    };
    auto issueV = [&](int slot, int st) {
      const char* p = vb + (size_t)st * 32768;
#pragma unroll
      for (int i = 0; i < 8; ++i) Vr[slot][i] = ldnt(p + i * 1024);
    };
    issueK(0, 0); issueV(0, 0); issueK(1, 1); issueV(1, 1);
    __builtin_amdgcn_sched_barrier(0);
    wg_barrier();
    if (wave == 0) STAMP(1);
    v4u qf[4];
    {
      float q0 = 0.f, q1 = 0.f;
#pragma unroll
      for (int j = 0; j < 4; ++j) { const v2f p = *(const v2f*)&sm.qpart[j][2 * lane]; q0 += p[0]; q1 += p[1]; }
      sm.qbf[wave][lane] = cvtpk(q0, q1);
      asm volatile("s_waitcnt lgkmcnt(0)" ::: "memory");
#pragma unroll
      for (int s = 0; s < 4; ++s) qf[s] = *(const v4u*)&sm.qbf[wave][16 * s + 4 * t];
    }
    float m = -INFINITY, l = 0.f;
    v4f O0 = {0.f, 0.f, 0.f, 0.f}, O1 = {0.f, 0.f, 0.f, 0.f};
    auto step = [&](int slot, int st, bool more) {
      v4f S0 = {0.f, 0.f, 0.f, 0.f}, S1 = {0.f, 0.f, 0.f, 0.f};
#pragma unroll
      for (int s = 0; s < 4; ++s) { S0 = mfma16(Kr[slot][s], qf[s], S0); S1 = mfma16(Kr[slot][4 + s], qf[s], S1); }
      if (more) issueK(slot, st + 2);
      float sc[8];
#pragma unroll
      for (int i = 0; i < 4; i += 2) {
        unsigned a = cvtpk(S0[i], S0[i + 1]), d = cvtpk(S1[i], S1[i + 1]);
        a = cvtpk(bflo(a) * SCALE, bfhi(a) * SCALE); d = cvtpk(bflo(d) * SCALE, bfhi(d) * SCALE);
        sc[i] = bflo(a); sc[i + 1] = bfhi(a); sc[4 + i] = bflo(d); sc[5 + i] = bfhi(d);
      }
      constexpr float C2 = LOG2E;
      float mx = fmaxf(fmaxf(fmaxf(sc[0], sc[1]), fmaxf(sc[2], sc[3])), fmaxf(fmaxf(sc[4], sc[5]), fmaxf(sc[6], sc[7])));
      mx = swap32_max(swap16_max(mx));
      const float mn = fmaxf(m, mx);
      const float alpha = __builtin_amdgcn_exp2f((m - mn) * C2);
      const float mo = mn * C2;
      m = mn;
      float p[8], ps = 0.f;
#pragma unroll
      for (int i = 0; i < 8; ++i) { p[i] = __builtin_amdgcn_exp2f(fmaf(sc[i], C2, -mo)); ps += p[i]; }
      l = fmaf(l, alpha, ps);
      O0 *= alpha; O1 *= alpha;
#pragma unroll
      for (int i = 0; i < 4; ++i) {
        O0 = mfma16(diag(cvtpk(p[i], p[i])), Vr[slot][i], O0);
        O1 = mfma16(diag(cvtpk(p[4 + i], p[4 + i])), Vr[slot][4 + i], O1);
      }
      if (more) issueV(slot, st + 2);
      __builtin_amdgcn_sched_barrier(0);
    };
    for (int st = 0; st < NSTEP - 2; st += 2) { step(0, st, true); step(1, st + 1, true); }
    step(0, NSTEP - 2, false); step(1, NSTEP - 1, false);
    if (wave == 0) STAMP(2);
    O0 += O1;
    l = swap32_sum(swap16_sum(l));
    const float mlog = m * LOG2E;
    if (lane < 32) *(v4f*)&sm.comb[wave][8 * c + 4 * t] = O0;
    if (lane == 0) { sm.comb[wave][DH] = mlog; sm.comb[wave][DH + 1] = l; }
    wg_barrier();
  } else {
    const int j = wave - 4;
    const uint16_t* xr = x + b * DM;
    const v4u xa = ldg(xr + 8 * lane);
    v4u xb2 = {0u, 0u, 0u, 0u};
    if (lane < 16) xb2 = ldg(xr + 512 + 8 * lane);
    const int rot = b % 5;
    v4u xq[5];
    int rr[5];
#pragma unroll
    for (int n = 0; n < 5; ++n) { int r = n + rot; r = r >= 5 ? r - 5 : r; rr[n] = r; xq[n] = ldg(xr + 160 * j + 40 * t + 8 * r); }
    const unsigned tag0 = __hip_atomic_load(epoch_p, __ATOMIC_RELAXED, __HIP_MEMORY_SCOPE_AGENT) + 1u;
    v4u W[40];
    {
      const char* p = wq + (size_t)(160 * j + 40 * t) * (NH * DH * 2) + h * (DH * 2) + c * 16;
#pragma unroll
      for (int n = 0; n < 5; ++n)
#pragma unroll
        for (int i = 0; i < 8; ++i) W[n * 8 + i] = ldg(p + (size_t)(8 * rr[n] + i) * (NH * DH * 2));
    }
    __builtin_amdgcn_sched_barrier(0);
    float xv[16];
#pragma unroll
    for (int i = 0; i < 4; ++i) { xv[2 * i] = bflo(xa[i]); xv[2 * i + 1] = bfhi(xa[i]); xv[8 + 2 * i] = bflo(xb2[i]); xv[9 + 2 * i] = bfhi(xb2[i]); }
    float m1, den1;
    ln_stats(xv, lane < 16, m1, den1);
    {
      v4f Q0 = {0.f, 0.f, 0.f, 0.f}, Q1 = {0.f, 0.f, 0.f, 0.f};
#pragma unroll
      for (int n = 0; n < 5; ++n)
#pragma unroll
        for (int i = 0; i < 4; ++i) {
          const float e0 = ln_apply(bflo(xq[n][i]), m1, den1), e1 = ln_apply(bfhi(xq[n][i]), m1, den1);
          Q0 = mfma16(diag(cvtpk(e0, e0)), W[n * 8 + 2 * i], Q0);
          Q1 = mfma16(diag(cvtpk(e1, e1)), W[n * 8 + 2 * i + 1], Q1);
        }
      Q0 += Q1;
      if (lane < 32) *(v4f*)&sm.qpart[j][8 * c + 4 * t] = Q0;
    }
    wg_barrier();
    float gw1[7], gb1[7], gw2[7];
    unsigned xcol;
    {
      unsigned r1[7], r2[7], r3[7];
#pragma unroll
      for (int i = 0; i < 7; ++i) {
        const int f = 256 * i + 64 * (i == 6 && j == 3 ? 0 : j) + lane;
        r1[i] = w1[f]; r2[i] = b1[f]; r3[i] = w2[f];
      }
      xcol = xr[COLS * h + (lane < COLS ? lane : 0)];
      r3[6] = j == 3 ? 0u : r3[6];
#pragma unroll
      for (int i = 0; i < 7; ++i) { gw1[i] = bflo(r1[i]); gb1[i] = bflo(r2[i]); gw2[i] = bflo(r3[i]); }
    }
    {
      const char* p = wo + (size_t)(DH * h + 32 * j + 8 * t) * (DM * 2) + c * 16;
#pragma unroll
      for (int rg = 0; rg < 8; ++rg)
#pragma unroll
        for (int cb = 0; cb < 5; ++cb) W[rg * 5 + cb] = ldg(p + rg * (DM * 2) + cb * 256);
    }
    __builtin_amdgcn_sched_barrier(0);
    float xn[16];
#pragma unroll
    for (int i = 0; i < 16; ++i) xn[i] = ln_apply(xv[i], m1, den1);
#pragma unroll
    for (int i = 8; i < 16; ++i) xn[i] = lane < 16 ? xn[i] : 0.f;
    float m2, den2;
    ln_stats(xn, lane < 16, m2, den2);
    const float hv = ln_apply(ln_apply(bflo(xcol), m1, den1), m2, den2);
    float gsum = 0.f;
#pragma unroll 4
    for (int col = 0; col < COLS; ++col) {
      const float hc = __builtin_bit_cast(float, __builtin_amdgcn_readlane(__builtin_bit_cast(int, hv), col));
      float acc = 0.f;
#pragma unroll
      for (int i = 0; i < 7; ++i) {
        const float u = rbf(rbf(hc * gw1[i]) + gb1[i]);
        const float z = u * fmaf(u * u, 0.044715f * 0.7978845608028654f * 2.f * LOG2E, 0.7978845608028654f * 2.f * LOG2E);
        const float ge = rbf(u * __builtin_amdgcn_rcpf(1.f + __builtin_amdgcn_exp2f(-z)));
        acc = fmaf(ge, gw2[i], acc);
      }
      acc = wave_sum(acc);
      gsum = lane == col ? acc : gsum;
    }
    if (lane < COLS) sm.gpart[j][lane] = gsum;
    if (lane == 0) sm.tag[j] = tag0;
    if (wave == 4) STAMP(3);
    wg_barrier();
    float mw[4], M = -INFINITY;
#pragma unroll
    for (int w = 0; w < 4; ++w) { mw[w] = sm.comb[w][DH]; M = fmaxf(M, mw[w]); }
    float L = 0.f, o[8];
#pragma unroll
    for (int i = 0; i < 8; ++i) o[i] = 0.f;
#pragma unroll
    for (int w = 0; w < 4; ++w) {
      const float wt = __builtin_amdgcn_exp2f(mw[w] - M);
      L = fmaf(wt, sm.comb[w][DH + 1], L);
      const v4f a0 = *(const v4f*)&sm.comb[w][32 * j + 8 * t], a1 = *(const v4f*)&sm.comb[w][32 * j + 8 * t + 4];
#pragma unroll
      for (int i = 0; i < 4; ++i) { o[i] = fmaf(wt, a0[i], o[i]); o[4 + i] = fmaf(wt, a1[i], o[4 + i]); }
    }
    const float inv = 1.0f / L;
    v4f acc[5];
#pragma unroll
    for (int cb = 0; cb < 5; ++cb) acc[cb] = v4f{0.f, 0.f, 0.f, 0.f};
#pragma unroll
    for (int rg = 0; rg < 8; ++rg) {
      const float ov = o[rg] * inv;
      const v4u a = diag(cvtpk(ov, ov));
#pragma unroll
      for (int cb = 0; cb < 5; ++cb) acc[cb] = mfma16(a, W[rg * 5 + cb], acc[cb]);
    }
    if (lane < 32) {
#pragma unroll
      for (int cb = 0; cb < 5; ++cb) *(v4f*)&sm.spart[j][128 * cb + 8 * c + 4 * t] = acc[cb];
    }
  }
  wg_barrier();
  if (wave >= 5) return;
  if (wave == 0) STAMP(4);
  const unsigned tag = sm.tag[0];
  const __amdgpu_buffer_rsrc_t rs = __builtin_amdgcn_make_buffer_rsrc(ws + WS_GRAN + (size_t)b * (NH * NH * COLS * 8), 0, NH * NH * COLS * 8, 0x00020000);
  {
    float s0 = 0.f, s1 = 0.f;
#pragma unroll
    for (int w = 0; w < 4; ++w) { const v2f p = *(const v2f*)&sm.spart[w][2 * tid]; s0 += p[0]; s1 += p[1]; }
    const int cons = tid / 10, cp = tid - cons * 10;
    v4u g = {__float_as_uint(s0), tag, __float_as_uint(s1), tag};
    __builtin_amdgcn_raw_buffer_store_b128(g, rs, (cons * NH + h) * (COLS * 8) + cp * 16, 0, 16);
  }
  {
    const int cp = 2 * wave + (lane >> 5), prod = lane & 31;
    const int col = COLS * h + 2 * cp;
    unsigned xw = *(const unsigned*)(x + b * DM + col);
    float y0 = 0.f, y1 = 0.f;
#pragma unroll
    for (int w = 0; w < 4; ++w) { y0 += sm.gpart[w][2 * cp]; y1 += sm.gpart[w][2 * cp + 1]; }
    const float bb = bflo(b2[0]);
    y0 = rbf(rbf(y0) + bb); y1 = rbf(rbf(y1) + bb);
    float g0 = rbf(y0 / (1.f + __expf(-y0))), g1 = rbf(y1 / (1.f + __expf(-y1)));
    asm volatile("" : "+v"(g0), "+v"(g1), "+v"(xw));
    const char* gp = ws + WS_GRAN + (size_t)b * (NH * NH * COLS * 8) + (h * NH + prod) * (COLS * 8) + cp * 16;
    v4u g;
    for (unsigned spins = 0; spins < SPIN_CAP; ++spins) {
      asm volatile("global_load_dwordx4 %0, %1, off sc1\n\ts_waitcnt vmcnt(0)" : "=&v"(g) : "v"(gp) : "memory");
      if (__all(g[1] == tag && g[3] == tag)) break;
      __builtin_amdgcn_s_sleep(1);
    }
    while ((int)((unsigned)__builtin_amdgcn_s_memrealtime() - tk0) < 15000) __builtin_amdgcn_s_sleep(1);
    const float s0 = rbf(half32_sum(__uint_as_float(g[0]))), s1 = rbf(half32_sum(__uint_as_float(g[2])));
    if (prod == 0) *(unsigned*)(out + b * DM + col) = cvtpk(bflo(xw) + rbf(s0 * g0), bfhi(xw) + rbf(s1 * g1));
    if (wave == 0) STAMP(5);
    if (h == 0 && tid == 0) __hip_atomic_store(epoch_p, tag, __ATOMIC_RELAXED, __HIP_MEMORY_SCOPE_AGENT);
  }
}

extern "C" void kernel_launch(void* const* d_in, const int* in_sizes, int n_in, void* d_out, int out_size, void* d_ws,
                              size_t ws_size, hipStream_t stream) {
  fused_decode<<<256, 512, 0, stream>>>((const uint16_t*)d_in[0], (const char*)d_in[1], (const char*)d_in[2],
                                         (const char*)d_in[3], (const char*)d_in[4], (const uint16_t*)d_in[6],
                                         (const uint16_t*)d_in[7], (const uint16_t*)d_in[8], (const uint16_t*)d_in[9],
                                         (uint16_t*)d_out, (char*)d_ws);
}
```

```cpp
#include <hip/hip_runtime.h>
#include <hip/hip_bf16.h>
#include <stdint.h>

#define GAS __attribute__((address_space(1)))
#define LAS __attribute__((address_space(3)))
typedef unsigned v4u __attribute__((ext_vector_type(4)));
typedef unsigned v2u __attribute__((ext_vector_type(2)));
typedef float v4f __attribute__((ext_vector_type(4)));
typedef float v2f __attribute__((ext_vector_type(2)));
typedef __bf16 bf16x8 __attribute__((ext_vector_type(8)));
typedef __bf16 bf16x2 __attribute__((ext_vector_type(2)));

#define DMIN 18
#define DSTEP 2
#define NLEV 6
#define LSH 6
#define TAU 856
#define TOFF 250
#define WQD 5
#define DSTART 24
#define NSTART 14
#define NRND 3
#define RN0 44
#define RNS 28
#define LSH2 4
#define CEN 24
#define WEARLY 0
#define O1DELAY 0
#define S2DELAY 0
#define PSLEEP 0
#define NG 2
#define PRE1 0
#define DRY 1
#define ABL 0
#define NPRE 3

constexpr int NB = 8, DM = 640, NH = 32, SK = 4096, DH = 128, FF = 1728;
constexpr float LOG2E = 1.4426950408889634f;
constexpr float SCALE = 0.08838834764831845f;
constexpr unsigned SPIN_CAP = 1u << 20;

constexpr size_t WS_CAL = 4096, WS_OBOX = 32768, OBOX_B = 512, WS_SBOX = WS_OBOX + 256 * OBOX_B, SBOX_B = 40 * NH * 128;

__device__ __forceinline__ v4f mfma16(v4u a, v4u b, v4f c) {
  return __builtin_amdgcn_mfma_f32_16x16x32_bf16(__builtin_bit_cast(bf16x8, a), __builtin_bit_cast(bf16x8, b), c, 0, 0, 0);
}
__device__ __forceinline__ unsigned cvtpk(float lo, float hi) {
  bf16x2 r = {(__bf16)lo, (__bf16)hi};
  return __builtin_bit_cast(unsigned, r);
}
__device__ __forceinline__ float bflo(unsigned u) { return __builtin_bit_cast(float, u << 16); }
__device__ __forceinline__ float bfhi(unsigned u) { return __builtin_bit_cast(float, u & 0xffff0000u); }
__device__ __forceinline__ float rbf(float x) { return (float)(__bf16)x; }
__device__ __forceinline__ v4u ldnt(const void* p) { return __builtin_nontemporal_load((const GAS v4u*)p); }
__device__ __forceinline__ unsigned ldnt16(const uint16_t* p) { return __builtin_nontemporal_load((const GAS uint16_t*)p); }
template <int D> __device__ __forceinline__ void dma4(unsigned lds, const char* base, unsigned o0, unsigned o1, unsigned o2, unsigned o3) {
  asm volatile("s_mov_b32 m0, %0\n\t"
               "s_waitcnt vmcnt(%6)\n\tglobal_load_lds_dwordx4 %2, %1 nt\n\t"
               "s_waitcnt vmcnt(%6)\n\tglobal_load_lds_dwordx4 %3, %1 offset:1024 nt\n\t"
               "s_waitcnt vmcnt(%6)\n\tglobal_load_lds_dwordx4 %4, %1 offset:2048 nt\n\t"
               "s_waitcnt vmcnt(%6)\n\tglobal_load_lds_dwordx4 %5, %1 offset:3072 nt"
               :: "s"(lds), "s"(base), "v"(o0), "v"(o1), "v"(o2), "v"(o3), "i"(D - 1) : "memory");
}
__device__ __forceinline__ void dma1(unsigned lds, const char* base, unsigned o) {
  asm volatile("s_mov_b32 m0, %0\n\ts_nop 0\n\tglobal_load_lds_dwordx4 %2, %1 nt" :: "s"(lds), "s"(base), "v"(o) : "memory");
}
typedef int v4i __attribute__((ext_vector_type(4)));
typedef short v4s __attribute__((ext_vector_type(4)));
__device__ __forceinline__ v4i mkrsrc(const void* p, unsigned bytes) {
  const unsigned long long a = (unsigned long long)p;
  return v4i{(int)__builtin_amdgcn_readfirstlane((unsigned)a), (int)(__builtin_amdgcn_readfirstlane((unsigned)(a >> 32)) & 0xffffu), (int)bytes, 0x00020000};
}
constexpr unsigned OOB = 0x7ff00000u;
template <int STRIDE> __device__ __forceinline__ void poll4(v4u (&r)[4], unsigned off, v4i rs) {
  asm volatile("buffer_load_dwordx4 %0, %4, %5, 0 offen sc1\n\tbuffer_load_dwordx4 %1, %4, %5, 0 offen offset:%6 sc1\n\t"
               "buffer_load_dwordx4 %2, %4, %5, 0 offen offset:%7 sc1\n\tbuffer_load_dwordx4 %3, %4, %5, 0 offen offset:%8 sc1\n\ts_waitcnt vmcnt(0)"
               : "+v"(r[0]), "+v"(r[1]), "+v"(r[2]), "+v"(r[3]) : "v"(off), "s"(rs), "i"(STRIDE), "i"(2 * STRIDE), "i"(3 * STRIDE) : "memory");
}
__device__ __forceinline__ bool tags_ok(const v4u (&r)[4], unsigned tag) {
  return r[0][1] == tag && r[0][3] == tag && r[1][1] == tag && r[1][3] == tag && r[2][1] == tag && r[2][3] == tag && r[3][1] == tag && r[3][3] == tag;
}
__device__ __forceinline__ unsigned lds_ld(const unsigned* p) { return __builtin_amdgcn_readfirstlane(*(const volatile LAS unsigned*)p); }
__device__ __forceinline__ void lds_st(unsigned* p, unsigned v) { *(volatile LAS unsigned*)p = v; }

template <int CTRL> __device__ __forceinline__ float dpp(float x) {
  return __builtin_bit_cast(float, __builtin_amdgcn_mov_dpp(__builtin_bit_cast(int, x), CTRL, 0xf, 0xf, true));
}
constexpr int XOR1 = 0xB1, XOR2 = 0x4E, XOR7 = 0x141, XOR15 = 0x140;
__device__ __forceinline__ float swap16_sum(float x) {
  auto s = __builtin_amdgcn_permlane16_swap(__float_as_uint(x), __float_as_uint(x), false, false);
  return __uint_as_float(s[0]) + __uint_as_float(s[1]);
}
__device__ __forceinline__ float swap32_sum(float x) {
  auto s = __builtin_amdgcn_permlane32_swap(__float_as_uint(x), __float_as_uint(x), false, false);
  return __uint_as_float(s[0]) + __uint_as_float(s[1]);
}
__device__ __forceinline__ float swap16_max(float x) {
  auto s = __builtin_amdgcn_permlane16_swap(__float_as_uint(x), __float_as_uint(x), false, false);
  return fmaxf(__uint_as_float(s[0]), __uint_as_float(s[1]));
}
__device__ __forceinline__ float swap32_max(float x) {
  auto s = __builtin_amdgcn_permlane32_swap(__float_as_uint(x), __float_as_uint(x), false, false);
  return fmaxf(__uint_as_float(s[0]), __uint_as_float(s[1]));
}
__device__ __forceinline__ float row16_sum(float x) {
  x += dpp<XOR1>(x); x += dpp<XOR2>(x); x += dpp<XOR7>(x); x += dpp<XOR15>(x); return x;
}
__device__ __forceinline__ float half32_sum(float x) { return swap16_sum(row16_sum(x)); }
__device__ __forceinline__ float wave_sum(float x) { return swap32_sum(half32_sum(x)); }

__device__ __forceinline__ float wave_max(float x) {
  x = fmaxf(x, dpp<XOR1>(x)); x = fmaxf(x, dpp<XOR2>(x)); x = fmaxf(x, dpp<XOR7>(x)); x = fmaxf(x, dpp<XOR15>(x));
  return swap32_max(swap16_max(x));
}
__device__ __forceinline__ void wg_barrier() {
  asm volatile("s_waitcnt lgkmcnt(0)" ::: "memory");
  __builtin_amdgcn_s_barrier();
  asm volatile("" ::: "memory");
}

__device__ __forceinline__ float ln_apply(float x, float m, float rden) { return rbf(rbf(x - m) * rden); }
template <int N, bool HALF>
__device__ __forceinline__ void ln_stats(const float (&v)[N], bool tail, float& m, float& den) {
  float s = 0.f, s2 = 0.f;
#pragma unroll
  for (int i = 0; i < N - 8; ++i) s += v[i];
#pragma unroll
  for (int i = N - 8; i < N; ++i) s2 += v[i];
  s += tail ? s2 : 0.f;
  s = HALF ? half32_sum(s) : wave_sum(s);
  const float mean = s * (1.0f / DM);
  float q = 0.f, q2 = 0.f;
#pragma unroll
  for (int i = 0; i < N - 8; ++i) { const float d = v[i] - mean; q = fmaf(d, d, q); }
#pragma unroll
  for (int i = N - 8; i < N; ++i) { const float d = v[i] - mean; q2 = fmaf(d, d, q2); }
  q += tail ? q2 : 0.f;
  q = HALF ? half32_sum(q) : wave_sum(q);
  const float var = rbf(q * (1.0f / DM));
  m = rbf(mean);
  den = __builtin_amdgcn_rcpf(rbf(__builtin_sqrtf(rbf(var + 1e-5f))));
}
__device__ __forceinline__ float rdlane(float x, int l) { return __builtin_bit_cast(float, __builtin_amdgcn_readlane(__builtin_bit_cast(int, x), l)); }

#define STAMP(k) do {} while (0)
constexpr int NSLOT = 9, TILE_B = 16384, NKT = SK / 64, NTILE = 2 * NKT, NCONS = 3;
constexpr int LASTC = (NTILE - 1) % NCONS;
constexpr int WSLOT = NTILE % NSLOT;
static_assert(WSLOT + 1 < NSLOT, "block spans two adjacent slots");
struct Smem {
  unsigned char ring[NSLOT * TILE_B];
  uint16_t sc[SK];
  float qpart[4][DH];
  unsigned qbf[NCONS][64];
  float ml[NCONS][2];
  float pad[2];
  float gpart[4][32];
  unsigned ctl[32];
};
constexpr int QFLAG = 0, WLAND = 4, FREED = 8, OFFW = 11, LANDED = 12, EOWN = 13, WGO = 14, TAG = 15, KDONE = 16, SENT = 20, GDONE = 24;

__device__ __forceinline__ void wait4(const unsigned* p) {
  for (;;) {
    const v4u f = *(const volatile LAS v4u*)p;
    if (__builtin_amdgcn_readfirstlane(f[0] & f[1] & f[2] & f[3])) break;
    __builtin_amdgcn_s_sleep(1);
  }
  asm volatile("" ::: "memory");
}

template <int D, typename F>
__device__ __forceinline__ void tile(int n, unsigned slot, const char* src, const unsigned (&of)[4], F& publish, const unsigned* ctl, v4u& cv, unsigned& e) {
  constexpr int PH = ((D + 3) / 4 + 1) & 1;
  dma4<D>(slot, src, of[0], of[1], of[2], of[3]);
  if (PH == 0) publish((16 * n + 4 - D) >> 3);
  dma4<D>(slot + 4096, src + 4096, of[0], of[1], of[2], of[3]);
  if (PH == 1) publish((16 * n + 8 - D) >> 3);
  dma4<D>(slot + 8192, src + 8192, of[0], of[1], of[2], of[3]);
  if (PH == 0) publish((16 * n + 12 - D) >> 3);
  cv = *(const volatile LAS v4u*)ctl;
  const unsigned now = (unsigned)__builtin_amdgcn_s_memrealtime();
  dma4<D>(slot + 12288, src + 12288, of[0], of[1], of[2], of[3]);
  if (PH == 1) publish((16 * n + 16 - D) >> 3);
  e = now - (unsigned)(((16 * n + 12 - D) * TAU) >> 8);
}

__global__ void __launch_bounds__(512, 2)
fused_decode(const uint16_t* __restrict__ x, const char* __restrict__ kc, const char* __restrict__ vc,
             const char* __restrict__ wq, const char* __restrict__ wo, const uint16_t* __restrict__ w1,
             const uint16_t* __restrict__ b1, const uint16_t* __restrict__ w2, const uint16_t* __restrict__ b2,
             uint16_t* __restrict__ out, char* ws) {
  __shared__ __attribute__((aligned(16))) Smem sm;
  const int tid = threadIdx.x, lane = tid & 63, wave = __builtin_amdgcn_readfirstlane(tid >> 6);
  const int bid = blockIdx.x;
  const int h = (bid & 7) * 4 + ((bid >> 3) & 3), b = bid >> 5;
  const int t = lane >> 4, c = lane & 15;
  v4u dmask;
#pragma unroll
  for (int i = 0; i < 4; ++i) dmask[i] = ((c & 7) >> 1) == i ? ((c & 1) ? 0xffff0000u : 0x0000ffffu) : 0u;
  auto diag = [&](unsigned dup) { v4u a; a[0] = dup & dmask[0]; a[1] = dup & dmask[1]; a[2] = dup & dmask[2]; a[3] = dup & dmask[3]; return a; };
  GAS unsigned* epoch_p = (GAS unsigned*)ws;

  if (wave == 0) {
    STAMP(0);
    const unsigned tstart = (unsigned)__builtin_amdgcn_s_memrealtime();
    __builtin_amdgcn_s_waitcnt(0x0F70);
    __builtin_amdgcn_s_setprio(3);
    const size_t hb = ((size_t)(b * NH + h)) * (SK * DH * 2);
    const char* src = kc + hb;
    unsigned of[4];
#pragma unroll
    for (int i = 0; i < 4; ++i) of[i] = t * 256 + ((c ^ (4 * i + t)) * 16);
    const unsigned ring0 = (unsigned)(size_t)(LAS unsigned char*)sm.ring;
    unsigned slot = ring0;
    int pub = 0;
    auto publish = [&](int v) { if (v > pub) { pub = v; lds_st(&sm.ctl[LANDED], (unsigned)v); } };
    dma4<63>(slot, src, of[0], of[1], of[2], of[3]);
    dma4<63>(slot + 4096, src + 4096, of[0], of[1], of[2], of[3]);
    wg_barrier();
    dma4<63>(slot + 8192, src + 8192, of[0], of[1], of[2], of[3]);
    dma4<63>(slot + 12288, src + 12288, of[0], of[1], of[2], of[3]);
    int cons = (NCONS - NSLOT % NCONS) % NCONS;
    v4u cv = {0u, 0u, 0u, 0u};
    unsigned e = tstart;
    for (int n = 1; n < NTILE; ++n) {
      src += TILE_B;
      if (n == 16) STAMP(9);
      if (n == 32) STAMP(10);
      if (n == NKT) { src = vc + hb; of[0] = of[1] = of[2] = of[3] = lane * 16; }
      slot += TILE_B; if (slot == ring0 + NSLOT * TILE_B) slot = ring0;
      cons = cons == NCONS - 1 ? 0 : cons + 1;
      if (n >= NSLOT) {
        const unsigned need = n - NSLOT + 1;
        if (__builtin_amdgcn_readfirstlane(cons == 0 ? cv[0] : cons == 1 ? cv[1] : cv[2]) < need && lds_ld(&sm.ctl[FREED + cons]) < need) {
          asm volatile("s_waitcnt vmcnt(0)" ::: "memory");
          publish(2 * n);
          while (lds_ld(&sm.ctl[FREED + cons]) < need) __builtin_amdgcn_s_sleep(1);
        }
      }
      const unsigned org = __builtin_amdgcn_readfirstlane(cv[3]);
      const int lev = org ? (int)(e - org) >> LSH2 : (int)(e - tstart - TOFF) >> LSH;
      if (WEARLY && n == NTILE - WEARLY) lds_st(&sm.ctl[WGO], 1u);
      if (NRND) lds_st(&sm.ctl[EOWN], e);
      const unsigned* cp = &sm.ctl[FREED];
      if (n < NSTART) tile<DSTART>(n, slot, src, of, publish, cp, cv, e);
      else if (lev <= 0) tile<DMIN>(n, slot, src, of, publish, cp, cv, e);
      else if (lev == 1 || NLEV == 2) tile<DMIN + DSTEP>(n, slot, src, of, publish, cp, cv, e);
      else if (lev == 2 || NLEV == 3) tile<DMIN + 2 * DSTEP>(n, slot, src, of, publish, cp, cv, e);
      else if (lev == 3 || NLEV == 4) tile<DMIN + 3 * DSTEP>(n, slot, src, of, publish, cp, cv, e);
      else if (lev == 4 || NLEV == 5) tile<DMIN + 4 * DSTEP>(n, slot, src, of, publish, cp, cv, e);
      else tile<DMIN + 5 * DSTEP>(n, slot, src, of, publish, cp, cv, e);
    }
    lds_st(&sm.ctl[WGO], 1u);
    asm volatile("s_waitcnt vmcnt(24)" ::: "memory");
    publish(2 * NTILE - 3);
    asm volatile("s_waitcnt vmcnt(16)" ::: "memory");
    publish(2 * NTILE - 2);
    asm volatile("s_waitcnt vmcnt(8)" ::: "memory");
    publish(2 * NTILE - 1);
    asm volatile("s_waitcnt vmcnt(0)" ::: "memory");
    publish(2 * NTILE);
    STAMP(6);
  } else if (wave < 4) {
    const int ci = wave - 1;
    if (wave == 1 && lane < 32) sm.ctl[lane] = (lane == KDONE + 3 || (lane >= SENT + NG && lane < SENT + 4)) ? 1u : 0u;
    wg_barrier();
    unsigned kof[4];
#pragma unroll
    for (int s = 0; s < 4; ++s) kof[s] = c * 256 + (((4 * s + t) ^ c) * 16);
    int slot = ci, n = ci;
    auto fetchK = [&](v4u (&Kr)[16]) {
      const unsigned char* tb = sm.ring + slot * TILE_B;
      while (lds_ld(&sm.ctl[LANDED]) < (unsigned)(2 * n + 2)) __builtin_amdgcn_s_sleep(1);
      asm volatile("" ::: "memory");
#pragma unroll
      for (int u = 0; u < 4; ++u)
#pragma unroll
        for (int s = 0; s < 4; ++s) Kr[4 * u + s] = *(const v4u*)(tb + u * 4096 + kof[s]);
      asm volatile("s_waitcnt lgkmcnt(0)" ::: "memory");
      lds_st(&sm.ctl[FREED + ci], n + 1);
#pragma unroll
      for (int i = 0; i < 16; ++i) asm volatile("" : "+v"(Kr[i]));
      slot += NCONS; slot = slot >= NSLOT ? slot - NSLOT : slot;
      n += NCONS;
    };
    v4u Kp[NPRE][16];
#pragma unroll
    for (int k = 0; k < NPRE; ++k) fetchK(Kp[k]);
    wait4(&sm.ctl[QFLAG]);
    if (wave == 1) STAMP(1);
    v4u qf[4];
    {
      float q0 = 0.f, q1 = 0.f;
#pragma unroll
      for (int j = 0; j < 4; ++j) { const v2f p = *(const v2f*)&sm.qpart[j][2 * lane]; q0 += p[0]; q1 += p[1]; }
      sm.qbf[ci][lane] = cvtpk(q0, q1);
      asm volatile("s_waitcnt lgkmcnt(0)" ::: "memory");
#pragma unroll
      for (int s = 0; s < 4; ++s) qf[s] = *(const v4u*)&sm.qbf[ci][16 * s + 4 * t];
    }
    float m = -INFINITY, l = 0.f;
    auto scoreK = [&](const v4u (&Kr)[16], int tile) {
      v4f S[4];
#pragma unroll
      for (int u = 0; u < 4; ++u) S[u] = v4f{0.f, 0.f, 0.f, 0.f};
#pragma unroll
      for (int s = 0; s < 4; ++s)
#pragma unroll
        for (int u = 0; u < 4; ++u) S[u] = mfma16(qf[s], Kr[4 * u + s], S[u]);
      const float sr = t == 0 ? S[0][0] : t == 1 ? S[1][0] : t == 2 ? S[2][0] : S[3][0];
      const unsigned sb = cvtpk(rbf(sr) * SCALE, 0.f);
      sm.sc[64 * tile + lane] = (uint16_t)sb;
      const float sv = bflo(sb) * LOG2E;
      const float mn = fmaxf(m, sv);
      l = fmaf(l, __builtin_amdgcn_exp2f(m - mn), __builtin_amdgcn_exp2f(sv - mn));
      m = mn;
    };
#pragma unroll
    for (int k = 0; k < NPRE; ++k) scoreK(Kp[k], ci + NCONS * k);
    while (n < NKT) {
      v4u Kr[16];
      fetchK(Kr);
      scoreK(Kr, n - NCONS);
    }
    {
      const float mw = wave_max(m);
      const float lw = wave_sum(l * __builtin_amdgcn_exp2f(m - mw));
      if (lane == 0) { sm.ml[ci][0] = mw; sm.ml[ci][1] = lw; }
      asm volatile("s_waitcnt lgkmcnt(0)" ::: "memory");
      lds_st(&sm.ctl[KDONE + ci], 1u);
      wait4(&sm.ctl[KDONE]);
    }
    float M = -INFINITY, L = 0.f;
#pragma unroll
    for (int w = 0; w < NCONS; ++w) M = fmaxf(M, sm.ml[w][0]);
#pragma unroll
    for (int w = 0; w < NCONS; ++w) L = fmaf(sm.ml[w][1], __builtin_amdgcn_exp2f(sm.ml[w][0] - M), L);
    const float invL = 1.0f / L;
    const unsigned tag = sm.ctl[TAG];
    if (wave == 1) STAMP(13);
    v4f O0 = {0.f, 0.f, 0.f, 0.f}, O1 = {0.f, 0.f, 0.f, 0.f};
    const unsigned vof = t * 1024 + c * 16;
    v4f oth = {0.f, 0.f, 0.f, 0.f};
    const __amdgpu_buffer_rsrc_t ro = __builtin_amdgcn_make_buffer_rsrc(ws + WS_OBOX + (size_t)(h * NB + b) * OBOX_B, 0, OBOX_B, 0x00020000);
    for (; n < NTILE; n += NCONS) {
      const unsigned char* tb = sm.ring + slot * TILE_B + vof;
      unsigned pd[16];
#pragma unroll
      for (int qd = 0; qd < 4; ++qd) {
        const v2u sv = *(const v2u*)&sm.sc[64 * (n - NKT) + 16 * qd + 4 * t];
#pragma unroll
        for (int i = 0; i < 2; ++i) {
          const float p0 = __builtin_amdgcn_exp2f(fmaf(bflo(sv[i]), LOG2E, -M)) * invL, p1 = __builtin_amdgcn_exp2f(fmaf(bfhi(sv[i]), LOG2E, -M)) * invL;
          pd[4 * qd + 2 * i] = cvtpk(p0, p0); pd[4 * qd + 2 * i + 1] = cvtpk(p1, p1);
        }
      }
#pragma unroll
      for (int hf = 0; hf < 2; ++hf) {
        v4u pa[8];
#pragma unroll
        for (int i = 0; i < 8; ++i) pa[i] = diag(pd[8 * hf + i]);
#pragma unroll
        for (int i = 0; i < 8; ++i) asm volatile("" : "+v"(pa[i]));
        if (hf == 1 && ci == LASTC) {
          const unsigned need = n == NTILE - 1 ? NTILE - 2 : 0;
          for (;;) {
            const v4u f = *(const volatile LAS v4u*)&sm.ctl[FREED];
            if (__builtin_amdgcn_readfirstlane(min(f[(LASTC + 1) % NCONS], f[(LASTC + 2) % NCONS])) >= need) break;
          }
          asm volatile("" ::: "memory");
          oth = *(const v4f*)&sm.qpart[(LASTC + 1) % NCONS][8 * c + 4 * (t & 1)] + *(const v4f*)&sm.qpart[(LASTC + 2) % NCONS][8 * c + 4 * (t & 1)];
          asm volatile("" : "+v"(oth));
        }
        while (lds_ld(&sm.ctl[LANDED]) < (unsigned)(2 * n + 1 + hf)) {}
        asm volatile("" ::: "memory");
        v4u Vr[8];
#pragma unroll
        for (int i = 0; i < 4; ++i) { Vr[i] = *(const v4u*)(tb + hf * 8192 + i * 256); Vr[4 + i] = *(const v4u*)(tb + hf * 8192 + 4096 + i * 256); }
#pragma unroll
        for (int i = 0; i < 4; ++i) { O0 = mfma16(pa[i], Vr[i], O0); O1 = mfma16(pa[4 + i], Vr[4 + i], O1); }
      }
      const v4f os = O0 + O1;
      if (ci == LASTC) {
        const v4f ot = os + oth;
        const v4u g = {cvtpk(ot[0], ot[1]), tag, cvtpk(ot[2], ot[3]), tag};
        __builtin_amdgcn_raw_buffer_store_b128(g, ro, (n == NTILE - 1 && lane < 32) ? (2 * c + t) * 16 : OOB, 0, 16);
      } else {
        if (lane < 32) *(v4f*)&sm.qpart[ci][8 * c + 4 * t] = os;
      }
      asm volatile("s_waitcnt lgkmcnt(0)" ::: "memory");
      lds_st(&sm.ctl[FREED + ci], n + 1);
      slot += NCONS; slot = slot >= NSLOT ? slot - NSLOT : slot;
    }
    if (ci == LASTC) STAMP(2);
  } else {
    const int j = wave - 4;
    wg_barrier();
    const uint16_t* xr = x + b * DM;
    const v4u xa = ldnt(xr + 8 * lane), xb = ldnt(xr + 512 + 8 * c);
    float m1, den1, xv[16];
    unsigned tag;
    {
      v4u xq[5];
#pragma unroll
      for (int n = 0; n < 5; ++n) xq[n] = ldnt(xr + 160 * j + 40 * t + 8 * n);
      unsigned ep = 0;
      if (j == 0) ep = __hip_atomic_load(epoch_p, __ATOMIC_RELAXED, __HIP_MEMORY_SCOPE_AGENT);
      v4u W[40];
      const char* p = wq + (size_t)(160 * j + 40 * t) * (NH * DH * 2) + h * (DH * 2) + c * 16;
#pragma unroll
      for (int i = 0; i < 40; ++i) {
        if (WQD < 40 && i >= WQD) { asm volatile("s_waitcnt vmcnt(%0)" :: "i"(WQD - 1) : "memory"); __builtin_amdgcn_sched_barrier(0); }
        W[i] = (ABL & 1) ? dmask : ldnt(p + (size_t)i * (NH * DH * 2));
        if (WQD < 40) __builtin_amdgcn_sched_barrier(0);
      }
      __builtin_amdgcn_sched_barrier(0);
#pragma unroll
      for (int i = 0; i < 4; ++i) { xv[2 * i] = bflo(xa[i]); xv[2 * i + 1] = bfhi(xa[i]); xv[8 + 2 * i] = bflo(xb[i]); xv[9 + 2 * i] = bfhi(xb[i]); }
      ln_stats<16, false>(xv, lane < 16, m1, den1);
      v4f Q0 = {0.f, 0.f, 0.f, 0.f}, Q1 = {0.f, 0.f, 0.f, 0.f};
#pragma unroll
      for (int i = 0; i < 20; ++i) {
        const unsigned w = xq[i >> 2][i & 3];
        const float e0 = rbf(bflo(w) - m1) * den1, e1 = rbf(bfhi(w) - m1) * den1;
        Q0 = mfma16(diag(cvtpk(e0, e0)), W[2 * i], Q0);
        Q1 = mfma16(diag(cvtpk(e1, e1)), W[2 * i + 1], Q1);
      }
      Q0 += Q1;
      if (lane < 32) *(v4f*)&sm.qpart[j][8 * c + 4 * t] = Q0;
      if (j == 0) lds_st(&sm.ctl[TAG], ep + 1u);
      asm volatile("s_waitcnt lgkmcnt(0)" ::: "memory");
      lds_st(&sm.ctl[QFLAG + j], 1u);
      if (wave == 4) STAMP(8);
    }
    const bool two = (h & 3) == 0;
    const int gcol = lane < 16 ? 16 * h + lane : 512 + 4 * h + c;
    unsigned xcol = 0, bb = 0;
    if (!(ABL & 4)) {
      const int fo = 8 * (54 * j + (lane < 54 ? lane : 0));
      const v4u r1 = ldnt(w1 + fo), r2 = ldnt(b1 + fo);
      v4u r3 = ldnt(w2 + fo);
      xcol = ldnt16(xr + (two ? gcol : 16 * h + c));
      bb = ldnt16(b2);
      r3 = lane < 54 ? r3 : v4u{0u, 0u, 0u, 0u};
      float gw1[8], gb1[8], gw2[8];
#pragma unroll
      for (int i = 0; i < 4; ++i) {
        gw1[2 * i] = bflo(r1[i]); gw1[2 * i + 1] = bfhi(r1[i]); gb1[2 * i] = bflo(r2[i]); gb1[2 * i + 1] = bfhi(r2[i]);
        gw2[2 * i] = bflo(r3[i]); gw2[2 * i + 1] = bfhi(r3[i]);
      }
      float m2, den2;
#pragma unroll
      for (int i = 0; i < 16; ++i) xv[i] = ln_apply(xv[i], m1, den1);
      ln_stats<16, false>(xv, lane < 16, m2, den2);
      const float hv = ln_apply(ln_apply(bflo(xcol), m1, den1), m2, den2);
      float gsum = 0.f;
      const int ncol = two ? 32 : 16;
#pragma unroll 2
      for (int col = 0; col < ncol; ++col) {
        const float hc = rdlane(hv, col);
        float acc = 0.f;
#pragma unroll
        for (int i = 0; i < 8; ++i) {
          const float u = rbf(rbf(hc * gw1[i]) + gb1[i]);
          const float z = u * fmaf(u * u, 0.044715f * 0.7978845608028654f * 2.f * LOG2E, 0.7978845608028654f * 2.f * LOG2E);
          const float ge = rbf(u * __builtin_amdgcn_rcpf(1.f + __builtin_amdgcn_exp2f(-z)));
          acc = fmaf(ge, gw2[i], acc);
        }
        acc = wave_sum(acc);
        gsum = lane == col ? acc : gsum;
      }
      if (lane < 32) sm.gpart[j][lane] = gsum;
    }
    asm volatile("s_waitcnt vmcnt(0) lgkmcnt(0)" ::: "memory");
    lds_st(&sm.ctl[GDONE + j], 1u);
    wait4(&sm.ctl[GDONE]);
    tag = sm.ctl[TAG];
    if (wave == 4) STAMP(3);
    if (NRND && j == 1) {
      const int sx = bid & 7, sy = bid >> 3;
      const bool contrib = ((sy + sx) & 7) == 1;
      for (int r = 0; r < NRND; ++r) {
        GAS unsigned long long* mp = (GAS unsigned long long*)(ws + WS_CAL + r * 4096);
        const unsigned n0 = 2 * (RN0 + r * RNS);
        while (lds_ld(&sm.ctl[LANDED]) < n0) __builtin_amdgcn_s_sleep(8);
        const unsigned eo = lds_ld(&sm.ctl[EOWN]);
        if (contrib && lane == 0) __hip_atomic_store(mp + (sx * 4 + (sy >> 3)) * 16, ((unsigned long long)tag << 32) | eo, __ATOMIC_RELAXED, __HIP_MEMORY_SCOPE_AGENT);
        while (lds_ld(&sm.ctl[LANDED]) < n0 + 12) __builtin_amdgcn_s_sleep(8);
        unsigned long long g = 0;
        if (lane < 32) g = __hip_atomic_load(mp + lane * 16, __ATOMIC_RELAXED, __HIP_MEMORY_SCOPE_AGENT);
        const bool valid = lane < 32 && (unsigned)(g >> 32) == tag;
        const int cnt = __builtin_popcount((unsigned)__ballot(valid));
        const float sum = wave_sum(valid ? (float)(int)((unsigned)g - eo) : 0.f);
        if (cnt >= 20) lds_st(&sm.ctl[OFFW], (eo + (int)(sum * __builtin_amdgcn_rcpf((float)cnt)) - CEN) | 1u);
      }
    }
    if (j < NG) {
      constexpr int NT = (5 + NG - 1) / NG;
      const v4i ro = mkrsrc(ws + WS_OBOX + (size_t)h * (NB * OBOX_B), NB * OBOX_B);
      const __amdgpu_buffer_rsrc_t rs = __builtin_amdgcn_make_buffer_rsrc(ws + WS_SBOX, 0, NB * SBOX_B, 0x00020000);
      const unsigned go = (c & 7) * OBOX_B + (2 * t + (c >> 3)) * 16;
      const unsigned so = (((c & 7) * 40 + 5 * b) * NH + h) * 128 + t * 32 + (c >> 3) * 16;
      int pass = DRY ? 0 : 1;
#pragma nounroll
      for (; pass < 2; ++pass) {
        asm volatile("" : "+s"(pass));
        v4u Wt[NT][4];
        auto readW = [&]() {
          asm volatile("" ::: "memory");
          const unsigned char* wl = sm.ring + WSLOT * TILE_B + (8 * t + (c >> 2)) * 160 + (c & 3) * 8;
#pragma unroll
          for (int e = 0; e < NT; ++e)
#pragma unroll
            for (int s = 0; s < 4; ++s) {
              const int ct = j + NG * e < 5 ? j + NG * e : 0;
              const v2u lo = __builtin_bit_cast(v2u, __builtin_amdgcn_ds_read_tr16_b64_v4i16((LAS v4s*)(wl + 32 * s * 160 + 32 * ct)));
              const v2u hi = __builtin_bit_cast(v2u, __builtin_amdgcn_ds_read_tr16_b64_v4i16((LAS v4s*)(wl + (32 * s + 4) * 160 + 32 * ct)));
              Wt[e][s] = v4u{lo[0], lo[1], hi[0], hi[1]};
            }
#pragma unroll
          for (int e = 0; e < NT; ++e)
#pragma unroll
            for (int s = 0; s < 4; ++s) asm volatile("" : "+v"(Wt[e][s]));
        };
        v4u r[4];
#pragma unroll
        for (int s = 0; s < 4; ++s) r[s] = v4u{0u, 0u, 0u, 0u};
        if (pass) {
          for (;;) {
            const v2u wf = *(const volatile LAS v2u*)&sm.ctl[WLAND];
            if (__builtin_amdgcn_readfirstlane(wf[0] & wf[1])) break;
            __builtin_amdgcn_s_sleep(1);
          }
        }
        readW();
        if (pass) { while (lds_ld(&sm.ctl[LANDED]) < (unsigned)(2 * NTILE)) {} if (O1DELAY) __builtin_amdgcn_s_sleep(O1DELAY); }
        bool ok = false;
        const unsigned cap = (pass && !(ABL & 64)) ? SPIN_CAP : 1u;
        for (unsigned spins = 0; spins < cap; ++spins) {
          if (!ok) { poll4<128>(r, (pass || PRE1) ? go : OOB, ro); ok = tags_ok(r, tag); }
          if (!__ballot(!ok)) break;
          if (PSLEEP) __builtin_amdgcn_s_sleep(PSLEEP);
        }
        if (wave == 4) STAMP(7);
        v4f D[NT];
#pragma unroll
        for (int e = 0; e < NT; ++e) D[e] = v4f{0.f, 0.f, 0.f, 0.f};
#pragma unroll
        for (int s = 0; s < 4; ++s) {
          const unsigned y0 = __builtin_amdgcn_mov_dpp(r[s][0], 0x128, 0xf, 0xf, true), y1 = __builtin_amdgcn_mov_dpp(r[s][2], 0x128, 0xf, 0xf, true);
          const v4u ob = c < 8 ? v4u{r[s][0], r[s][2], y0, y1} : v4u{y0, y1, r[s][0], r[s][2]};
#pragma unroll
          for (int e = 0; e < NT; ++e) D[e] = mfma16(Wt[e][s], ob, D[e]);
        }
#pragma unroll
        for (int e = 0; e < NT; ++e) {
          const v4u g = {__float_as_uint(c < 8 ? D[e][0] : D[e][2]), tag, __float_as_uint(c < 8 ? D[e][1] : D[e][3]), tag};
          __builtin_amdgcn_raw_buffer_store_b128(g, rs, (pass && j + NG * e < 5) ? so + (j + NG * e) * (NH * 128) : OOB, 0, 16);
        }
        lds_st(&sm.ctl[SENT + j], pass);
        if (wave == 4) STAMP(4);
      }
    } else if (j >= 2) {
      const int e = 3 - j, ct = e ? 32 + (h >> 2) : h, cp = lane & 7;
      float y0 = 0.f, y1 = 0.f;
#pragma unroll
      for (int w = 0; w < 4; ++w) { y0 += sm.gpart[w][16 * e + 2 * cp]; y1 += sm.gpart[w][16 * e + 2 * cp + 1]; }
      y0 = rbf(rbf(y0) + bflo(bb)); y1 = rbf(rbf(y1) + bflo(bb));
      const float g0 = rbf(y0 / (1.f + __expf(-y0))), g1 = rbf(y1 / (1.f + __expf(-y1)));
      const float x0 = bflo(__shfl(xcol, 16 * e + 2 * cp)), x1 = bflo(__shfl(xcol, 16 * e + 2 * cp + 1));
      const v4i rs = mkrsrc(ws + WS_SBOX + (size_t)(b * 40 + ct) * (NH * 128), NH * 128);
      const __amdgpu_buffer_rsrc_t rout = __builtin_amdgcn_make_buffer_rsrc(out, 0, NB * DM * 2, 0x00020000);
      unsigned wof[5];
#pragma unroll
      for (int p = 0; p < 5; ++p) {
        const unsigned id = 64 * p + lane, row = id / 10;
        wof[p] = row * (DM * 2) + (id - 10 * row) * 16;
        asm volatile("" : "+v"(wof[p]));
      }
      const char* wb = wo + (size_t)(DH * h + e * 64) * (DM * 2) + b * 160;
      const unsigned wdst = (unsigned)(size_t)(LAS unsigned char*)sm.ring + WSLOT * TILE_B + e * 64 * 160;
      int pass = DRY ? 0 : 1;
#pragma nounroll
      for (; pass < 2; ++pass) {
        asm volatile("" : "+s"(pass));
        if (pass) {
          while (!lds_ld(&sm.ctl[WGO])) __builtin_amdgcn_s_sleep(1);
          while (lds_ld(&sm.ctl[FREED + (NTILE - NSLOT) % NCONS]) < (unsigned)(NTILE - NSLOT + 1) ||
                 lds_ld(&sm.ctl[FREED + (NTILE - NSLOT + 1) % NCONS]) < (unsigned)(NTILE - NSLOT + 2)) {}
#pragma unroll
          for (int r = 0; r < 2; ++r)
#pragma unroll
            for (int p = 0; p < 5; ++p) dma1(wdst + (5 * r + p) * 1024, wb + r * 32 * (DM * 2), wof[p]);
          asm volatile("s_waitcnt vmcnt(0)" ::: "memory");
          lds_st(&sm.ctl[WLAND + e], 1u);
          if (wave == 7) STAMP(11);
          if (e && !two) break;
          wait4(&sm.ctl[SENT]);
          if (S2DELAY) __builtin_amdgcn_s_sleep(S2DELAY);
        }
        v4u r[4];
#pragma unroll
        for (int s = 0; s < 4; ++s) r[s] = v4u{0u, 0u, 0u, 0u};
        bool ok = false;
        const unsigned cap = (pass && !(ABL & 64)) ? SPIN_CAP : 1u;
        for (unsigned spins = 0; spins < cap; ++spins) {
          if (!ok) { poll4<1024>(r, pass ? lane * 16 : OOB, rs); ok = tags_ok(r, tag); }
          if (!__ballot(!ok)) break;
          if (PSLEEP) __builtin_amdgcn_s_sleep(PSLEEP);
        }
        float s0 = (__uint_as_float(r[0][0]) + __uint_as_float(r[1][0])) + (__uint_as_float(r[2][0]) + __uint_as_float(r[3][0]));
        float s1 = (__uint_as_float(r[0][2]) + __uint_as_float(r[1][2])) + (__uint_as_float(r[2][2]) + __uint_as_float(r[3][2]));
        s0 += dpp<0x128>(s0); s1 += dpp<0x128>(s1);
        s0 = rbf(swap32_sum(swap16_sum(s0))); s1 = rbf(swap32_sum(swap16_sum(s1)));
        const unsigned res = cvtpk(x0 + rbf(s0 * g0), x1 + rbf(s1 * g1));
        __builtin_amdgcn_raw_buffer_store_b32(res, rout, (pass && lane < 8) ? (b * DM + 16 * ct + 2 * cp) * 2 : OOB, 0, 0);
      }
      if (bid == 0 && tid == 448) __hip_atomic_store(epoch_p, tag, __ATOMIC_RELAXED, __HIP_MEMORY_SCOPE_AGENT);
    }
  }
}

extern "C" void kernel_launch(void* const* d_in, const int* in_sizes, int n_in, void* d_out, int out_size, void* d_ws,
                              size_t ws_size, hipStream_t stream) {
  fused_decode<<<256, 512, 0, stream>>>((const uint16_t*)d_in[0], (const char*)d_in[1], (const char*)d_in[2],
                                         (const char*)d_in[3], (const char*)d_in[4], (const uint16_t*)d_in[6],
                                         (const uint16_t*)d_in[7], (const uint16_t*)d_in[8], (const uint16_t*)d_in[9],
                                         (uint16_t*)d_out, (char*)d_ws);
}
```

```cpp
#include <hip/hip_runtime.h>
#include <hip/hip_bf16.h>
#include <stdint.h>

#define GAS __attribute__((address_space(1)))
#define LAS __attribute__((address_space(3)))
typedef unsigned v4u __attribute__((ext_vector_type(4)));
typedef unsigned v2u __attribute__((ext_vector_type(2)));
typedef float v4f __attribute__((ext_vector_type(4)));
typedef float v2f __attribute__((ext_vector_type(2)));
typedef __bf16 bf16x8 __attribute__((ext_vector_type(8)));
typedef __bf16 bf16x2 __attribute__((ext_vector_type(2)));

constexpr int DMIN = 18, DSTEP = 4;
constexpr int LSH = 6, LSH2 = 4;
constexpr int TAU = 870;
constexpr int TOFF = 140;
constexpr int CEN = 32;
constexpr int JUMP = 70;
constexpr int DSTART = 24, NSTART = 14;
constexpr int WQD = 5;
constexpr int NRND = 3, RN0 = 44, RNS = 30, RDLY = 8;
constexpr int S2DELAY = 16;
constexpr int LSPLIT = 12, NFIRST = 2, NPRE = 3, NG = 2, CSTR = 4, LNAT = 2 * WQD - 1;

constexpr int NB = 8, DM = 640, NH = 32, SK = 4096, DH = 128;
constexpr float LOG2E = 1.4426950408889634f;
constexpr float SCALE = 0.08838834764831845f;
constexpr unsigned SPIN_CAP = 1u << 20;

constexpr size_t WS_CAL = 4096, WS_OBOX = 65536, OBOX_B = 512, WS_SBOX = WS_OBOX + 256 * OBOX_B, SBOX_B = 40 * NH * 128;

__device__ __forceinline__ v4f mfma16(v4u a, v4u b, v4f c) {
  return __builtin_amdgcn_mfma_f32_16x16x32_bf16(__builtin_bit_cast(bf16x8, a), __builtin_bit_cast(bf16x8, b), c, 0, 0, 0);
}
__device__ __forceinline__ unsigned cvtpk(float lo, float hi) {
  bf16x2 r = {(__bf16)lo, (__bf16)hi};
  return __builtin_bit_cast(unsigned, r);
}
__device__ __forceinline__ float bflo(unsigned u) { return __builtin_bit_cast(float, u << 16); }
__device__ __forceinline__ float bfhi(unsigned u) { return __builtin_bit_cast(float, u & 0xffff0000u); }
__device__ __forceinline__ float rbf(float x) { return (float)(__bf16)x; }
__device__ __forceinline__ float expn(float x) { return __builtin_amdgcn_exp2f(x * LOG2E); }
__device__ __forceinline__ v4u ldnt(const void* p) { return __builtin_nontemporal_load((const GAS v4u*)p); }
__device__ __forceinline__ v4u ldg(const void* p) { return *(const GAS v4u*)p; }
template <int D> __device__ __forceinline__ void dma4(unsigned lds, const char* base, unsigned o0, unsigned o1, unsigned o2, unsigned o3) {
  asm volatile("s_mov_b32 m0, %0\n\t"
               "s_waitcnt vmcnt(%6)\n\tglobal_load_lds_dwordx4 %2, %1 nt\n\t"
               "s_waitcnt vmcnt(%6)\n\tglobal_load_lds_dwordx4 %3, %1 offset:1024 nt\n\t"
               "s_waitcnt vmcnt(%6)\n\tglobal_load_lds_dwordx4 %4, %1 offset:2048 nt\n\t"
               "s_waitcnt vmcnt(%6)\n\tglobal_load_lds_dwordx4 %5, %1 offset:3072 nt"
               :: "s"(lds), "s"(base), "v"(o0), "v"(o1), "v"(o2), "v"(o3), "i"(D - 1) : "memory");
}
__device__ __forceinline__ void dma1(unsigned lds, const char* base, unsigned o) {
  asm volatile("s_mov_b32 m0, %0\n\ts_nop 0\n\tglobal_load_lds_dwordx4 %2, %1 nt" :: "s"(lds), "s"(base), "v"(o) : "memory");
}
typedef int v4i __attribute__((ext_vector_type(4)));
typedef short v4s __attribute__((ext_vector_type(4)));
__device__ __forceinline__ v4i mkrsrc(const void* p, unsigned bytes) {
  const unsigned long long a = (unsigned long long)p;
  return v4i{(int)__builtin_amdgcn_readfirstlane((unsigned)a), (int)(__builtin_amdgcn_readfirstlane((unsigned)(a >> 32)) & 0xffffu), (int)bytes, 0x00020000};
}
constexpr unsigned OOB = 0x7ff00000u;
template <int STRIDE> __device__ __forceinline__ void poll4(v4u (&r)[4], unsigned off, v4i rs) {
  asm volatile("buffer_load_dwordx4 %0, %4, %5, 0 offen sc1\n\tbuffer_load_dwordx4 %1, %4, %5, 0 offen offset:%6 sc1\n\t"
               "buffer_load_dwordx4 %2, %4, %5, 0 offen offset:%7 sc1\n\tbuffer_load_dwordx4 %3, %4, %5, 0 offen offset:%8 sc1\n\ts_waitcnt vmcnt(0)"
               : "+v"(r[0]), "+v"(r[1]), "+v"(r[2]), "+v"(r[3]) : "v"(off), "s"(rs), "i"(STRIDE), "i"(2 * STRIDE), "i"(3 * STRIDE) : "memory");
}
__device__ __forceinline__ bool tags_ok(const v4u (&r)[4], unsigned tag) {
  return r[0][1] == tag && r[0][3] == tag && r[1][1] == tag && r[1][3] == tag && r[2][1] == tag && r[2][3] == tag && r[3][1] == tag && r[3][3] == tag;
}
template <int STRIDE> __device__ __forceinline__ void gather4(v4u (&r)[4], unsigned off, v4i rs, unsigned tag) {
#pragma unroll
  for (int s = 0; s < 4; ++s) r[s] = v4u{0u, 0u, 0u, 0u};
  bool ok = false;
  for (unsigned spins = 0; spins < SPIN_CAP; ++spins) {
    if (!ok) { poll4<STRIDE>(r, off, rs); ok = tags_ok(r, tag); }
    if (!__ballot(!ok)) break;
  }
}
__device__ __forceinline__ unsigned lds_ld(const unsigned* p) { return __builtin_amdgcn_readfirstlane(*(const volatile LAS unsigned*)p); }
__device__ __forceinline__ void lds_st(unsigned* p, unsigned v) { *(volatile LAS unsigned*)p = v; }

template <int CTRL> __device__ __forceinline__ float dpp(float x) {
  return __builtin_bit_cast(float, __builtin_amdgcn_mov_dpp(__builtin_bit_cast(int, x), CTRL, 0xf, 0xf, true));
}
constexpr int XOR1 = 0xB1, XOR2 = 0x4E, XOR7 = 0x141, XOR15 = 0x140, ROR8 = 0x128;
__device__ __forceinline__ float swap16_sum(float x) {
  auto s = __builtin_amdgcn_permlane16_swap(__float_as_uint(x), __float_as_uint(x), false, false);
  return __uint_as_float(s[0]) + __uint_as_float(s[1]);
}
__device__ __forceinline__ float swap32_sum(float x) {
  auto s = __builtin_amdgcn_permlane32_swap(__float_as_uint(x), __float_as_uint(x), false, false);
  return __uint_as_float(s[0]) + __uint_as_float(s[1]);
}
__device__ __forceinline__ float swap16_max(float x) {
  auto s = __builtin_amdgcn_permlane16_swap(__float_as_uint(x), __float_as_uint(x), false, false);
  return fmaxf(__uint_as_float(s[0]), __uint_as_float(s[1]));
}
__device__ __forceinline__ float swap32_max(float x) {
  auto s = __builtin_amdgcn_permlane32_swap(__float_as_uint(x), __float_as_uint(x), false, false);
  return fmaxf(__uint_as_float(s[0]), __uint_as_float(s[1]));
}
__device__ __forceinline__ float row16_sum(float x) {
  x += dpp<XOR1>(x); x += dpp<XOR2>(x); x += dpp<XOR7>(x); x += dpp<XOR15>(x); return x;
}
__device__ __forceinline__ float wave_sum(float x) { return swap32_sum(swap16_sum(row16_sum(x))); }

__device__ __forceinline__ float wave_max(float x) {
  x = fmaxf(x, dpp<XOR1>(x)); x = fmaxf(x, dpp<XOR2>(x)); x = fmaxf(x, dpp<XOR7>(x)); x = fmaxf(x, dpp<XOR15>(x));
  return swap32_max(swap16_max(x));
}
__device__ __forceinline__ void wg_barrier() {
  asm volatile("s_waitcnt lgkmcnt(0)" ::: "memory");
  __builtin_amdgcn_s_barrier();
  asm volatile("" ::: "memory");
}


__device__ __forceinline__ float ln_apply(float x, float m, float rden) { return rbf(rbf(x - m) * rden); }
template <int N>
__device__ __forceinline__ void ln_stats(const float (&v)[N], bool tail, float& m, float& den) {
  float s = 0.f, s2 = 0.f;
#pragma unroll
  for (int i = 0; i < N - 8; ++i) s += v[i];
#pragma unroll
  for (int i = N - 8; i < N; ++i) s2 += v[i];
  s += tail ? s2 : 0.f;
  s = wave_sum(s);
  const float mean = s * (1.0f / DM);
  float q = 0.f, q2 = 0.f;
#pragma unroll
  for (int i = 0; i < N - 8; ++i) { const float d = v[i] - mean; q = fmaf(d, d, q); }
#pragma unroll
  for (int i = N - 8; i < N; ++i) { const float d = v[i] - mean; q2 = fmaf(d, d, q2); }
  q += tail ? q2 : 0.f;
  q = wave_sum(q);
  const float var = rbf(q * (1.0f / DM));
  m = rbf(mean);
  den = __builtin_amdgcn_rcpf(rbf(__builtin_sqrtf(rbf(var + 1e-5f))));
}
__device__ __forceinline__ float rdlane(float x, int l) { return __builtin_bit_cast(float, __builtin_amdgcn_readlane(__builtin_bit_cast(int, x), l)); }

constexpr int NSLOT = 9, TILE_B = 16384, NKT = SK / 64, NTILE = 2 * NKT, NCONS = 3;
constexpr int LASTC = (NTILE - 1) % NCONS;
constexpr int WSLOT = NTILE % NSLOT;
struct Smem {
  unsigned char ring[NSLOT * TILE_B];
  uint16_t sc[SK];
  float qpart[4][DH];
  unsigned qbf[NCONS][64];
  float ml[NCONS][2];
  float pad[2];
  float gpart[4][32];
  unsigned ctl[32];
};
constexpr int QFLAG = 0, WLAND = 4, BIAS = 6, FREED = 8, OFFW = 11, LANDED = 12, EOWN = 13, WGO = 14, TAG = 15, KDONE = 16, SENT = 20, GDONE = 24, TAGOK = 28, LGO = 29;

__device__ __forceinline__ void wait4(const unsigned* p) {
  for (;;) {
    const v4u f = *(const volatile LAS v4u*)p;
    if (__builtin_amdgcn_readfirstlane(f[0] & f[1] & f[2] & f[3])) break;
    __builtin_amdgcn_s_sleep(1);
  }
  asm volatile("" ::: "memory");
}

template <int D, typename F>
__device__ __forceinline__ void tile(int n, unsigned slot, const char* src, const unsigned (&of)[4], F& publish, const unsigned* ctl, v4u& cv, unsigned& e) {
  constexpr int PH = ((D + 3) / 4 + 1) & 1;
  dma4<D>(slot, src, of[0], of[1], of[2], of[3]);
  if (PH == 0) publish((16 * n + 4 - D) >> 3);
  dma4<D>(slot + 4096, src + 4096, of[0], of[1], of[2], of[3]);
  if (PH == 1) publish((16 * n + 8 - D) >> 3);
  dma4<D>(slot + 8192, src + 8192, of[0], of[1], of[2], of[3]);
  if (PH == 0) publish((16 * n + 12 - D) >> 3);
  cv = *(const volatile LAS v4u*)ctl;
  const unsigned now = (unsigned)__builtin_amdgcn_s_memrealtime();
  dma4<D>(slot + 12288, src + 12288, of[0], of[1], of[2], of[3]);
  if (PH == 1) publish((16 * n + 16 - D) >> 3);
  e = now - (unsigned)(((16 * n + 12 - D) * TAU) >> 8);
}

__global__ void __launch_bounds__(512, 2)
fused_decode(const uint16_t* __restrict__ x, const char* __restrict__ kc, const char* __restrict__ vc,
             const char* __restrict__ wq, const char* __restrict__ wo, const uint16_t* __restrict__ w1,
             const uint16_t* __restrict__ b1, const uint16_t* __restrict__ w2, const uint16_t* __restrict__ b2,
             uint16_t* __restrict__ out, char* ws) {
  __shared__ __attribute__((aligned(16))) Smem sm;
  const unsigned tstart = (unsigned)__builtin_amdgcn_s_memrealtime();
  __builtin_amdgcn_sched_barrier(0);
  const int tid = threadIdx.x, lane = tid & 63, wave = __builtin_amdgcn_readfirstlane(tid >> 6);
  const int bid = blockIdx.x;
  const int h = (bid & 7) * 4 + ((bid >> 3) & 3), b = bid >> 5;
  const int t = lane >> 4, c = lane & 15;
  const size_t hb = ((size_t)(b * NH + h)) * (SK * DH * 2);
  const unsigned ring0 = (unsigned)(size_t)(LAS unsigned char*)sm.ring;
  unsigned of[4];
#pragma unroll
  for (int i = 0; i < 4; ++i) of[i] = t * 256 + ((c ^ (4 * i + t)) * 16);
  {
    const char* p = kc + hb;
    unsigned m = ring0;
    asm volatile("s_cmp_lg_u32 %6, 0\n\ts_cbranch_scc1 2f\n"
                 "1:\n\ts_mov_b32 m0, %0\n\ts_add_u32 %0, %0, 0x1000\n\t"
                 "global_load_lds_dwordx4 %2, %1 nt\n\tglobal_load_lds_dwordx4 %3, %1 offset:1024 nt\n\t"
                 "global_load_lds_dwordx4 %4, %1 offset:2048 nt\n\tglobal_load_lds_dwordx4 %5, %1 offset:3072 nt\n\t"
                 "v_add_u32 %2, 0x1000, %2\n\tv_add_u32 %3, 0x1000, %3\n\tv_add_u32 %4, 0x1000, %4\n\tv_add_u32 %5, 0x1000, %5\n\t"
                 "s_cmp_lt_u32 %0, %7\n\ts_cbranch_scc1 1b\n"
                 "2:"
                 : "+&s"(m), "+&s"(p), "+&v"(of[0]), "+&v"(of[1]), "+&v"(of[2]), "+&v"(of[3])
                 : "s"(wave), "s"(ring0 + NFIRST * TILE_B) : "memory", "scc");
  }
  v4u dmask;
#pragma unroll
  for (int i = 0; i < 4; ++i) dmask[i] = ((c & 7) >> 1) == i ? ((c & 1) ? 0xffff0000u : 0x0000ffffu) : 0u;
  auto diag = [&](unsigned dup) { v4u a; a[0] = dup & dmask[0]; a[1] = dup & dmask[1]; a[2] = dup & dmask[2]; a[3] = dup & dmask[3]; return a; };
  GAS unsigned* epoch_p = (GAS unsigned*)ws;

  if (wave == 0) {
    __builtin_amdgcn_s_setprio(3);
    const char* src = kc + hb + (NFIRST - 1) * TILE_B;
#pragma unroll
    for (int i = 0; i < 4; ++i) of[i] -= NFIRST * TILE_B;
    unsigned slot = ring0 + (NFIRST - 1) * TILE_B;
    int pub = 0;
    auto publish = [&](int v) { if (v > pub) { pub = v; lds_st(&sm.ctl[LANDED], (unsigned)v); } };
    wg_barrier();
    int cons = (NCONS - NSLOT % NCONS + NFIRST - 1) % NCONS;
    v4u cv = {0u, 0u, 0u, 0u};
    unsigned eprev = tstart, bias = 0;
    unsigned e = tstart;
    for (int n = NFIRST; n < NTILE - 1; ++n) {
      src += TILE_B;
      if (n == NKT) { src = vc + hb; of[0] = of[1] = of[2] = of[3] = lane * 16; }
      slot += TILE_B; if (slot == ring0 + NSLOT * TILE_B) slot = ring0;
      cons = cons == NCONS - 1 ? 0 : cons + 1;
      if (n >= NSLOT) {
        const unsigned need = n - NSLOT + 1;
        if (__builtin_amdgcn_readfirstlane(cons == 0 ? cv[0] : cons == 1 ? cv[1] : cv[2]) < need && lds_ld(&sm.ctl[FREED + cons]) < need) {
          asm volatile("s_waitcnt vmcnt(0)" ::: "memory");
          publish(2 * n);
          while (lds_ld(&sm.ctl[FREED + cons]) < need) __builtin_amdgcn_s_sleep(1);
        }
      }
      const unsigned org = __builtin_amdgcn_readfirstlane(cv[3]);
      if (n > NSTART + 8 && (int)(e - eprev) > JUMP) { bias += e - eprev; lds_st(&sm.ctl[BIAS], bias); }
      eprev = e;
      const int lev = (int)(e - bias - (org ? org : tstart + TOFF)) >> ((org & 2) ? LSH2 : LSH);
      lds_st(&sm.ctl[EOWN], e);
      const unsigned* cp = &sm.ctl[FREED];
      if (n < NSTART) tile<DSTART>(n, slot, src, of, publish, cp, cv, e);
      else if (lev <= 0) tile<DMIN>(n, slot, src, of, publish, cp, cv, e);
      else if (lev == 1) tile<DMIN + DSTEP>(n, slot, src, of, publish, cp, cv, e);
      else if (lev == 2) tile<DMIN + 2 * DSTEP>(n, slot, src, of, publish, cp, cv, e);
      else if (lev == 3) tile<DMIN + 3 * DSTEP>(n, slot, src, of, publish, cp, cv, e);
      else if (lev == 4) tile<DMIN + 4 * DSTEP>(n, slot, src, of, publish, cp, cv, e);
      else tile<DMIN + 5 * DSTEP>(n, slot, src, of, publish, cp, cv, e);
    }
    lds_st(&sm.ctl[LGO], 1u);
    asm volatile("s_waitcnt vmcnt(32)" ::: "memory");
    publish(2 * NTILE - 6);
    asm volatile("s_waitcnt vmcnt(24)" ::: "memory");
    publish(2 * NTILE - 5);
    asm volatile("s_waitcnt vmcnt(16)" ::: "memory");
    publish(2 * NTILE - 4);
    asm volatile("s_waitcnt vmcnt(8)" ::: "memory");
    publish(2 * NTILE - 3);
    asm volatile("s_waitcnt vmcnt(0)" ::: "memory");
    publish(2 * NTILE - 2);
  } else if (wave < 4) {
    const int ci = wave - 1;
    if (wave == 1 && lane < 32) sm.ctl[lane] = (lane == KDONE + 3 || (lane >= SENT + NG && lane < SENT + 4)) ? 1u : 0u;
    wg_barrier();
    unsigned tag0 = 0;
    GAS unsigned long long* sp = (GAS unsigned long long*)(ws + WS_CAL + NRND * 4096);
    if (wave == 3) {
      tag0 = __builtin_amdgcn_readfirstlane(__hip_atomic_load(epoch_p, __ATOMIC_RELAXED, __HIP_MEMORY_SCOPE_AGENT)) * 0x9E3779B1u + 0x7F4A7C15u;
      lds_st(&sm.ctl[TAG], tag0);
      lds_st(&sm.ctl[TAGOK], 1u);
      if (((((bid >> 3) + bid) & 7) == 1) && lane == 0)
        __hip_atomic_store(sp + ((bid & 7) * 4 + (bid >> 6)) * CSTR, ((unsigned long long)tag0 << 32) | tstart, __ATOMIC_RELAXED, __HIP_MEMORY_SCOPE_AGENT);
    }
    unsigned kof[4];
#pragma unroll
    for (int s = 0; s < 4; ++s) kof[s] = c * 256 + (((4 * s + t) ^ c) * 16);
    int slot = ci, n = ci;
    auto fetchK = [&](v4u (&Kr)[16]) {
      const unsigned char* tb = sm.ring + slot * TILE_B;
      while (lds_ld(&sm.ctl[LANDED]) < (unsigned)(2 * n + 2)) __builtin_amdgcn_s_sleep(1);
      asm volatile("" ::: "memory");
#pragma unroll
      for (int u = 0; u < 4; ++u)
#pragma unroll
        for (int s = 0; s < 4; ++s) Kr[4 * u + s] = *(const v4u*)(tb + u * 4096 + kof[s]);
      asm volatile("s_waitcnt lgkmcnt(0)" ::: "memory");
      lds_st(&sm.ctl[FREED + ci], n + 1);
#pragma unroll
      for (int i = 0; i < 16; ++i) asm volatile("" : "+v"(Kr[i]));
      slot += NCONS; slot = slot >= NSLOT ? slot - NSLOT : slot;
      n += NCONS;
    };
    v4u Kp[NPRE][16];
#pragma unroll
    for (int k = 0; k < NPRE; ++k) {
      fetchK(Kp[k]);
      if (k == NPRE - 1 && wave == 3) {
        unsigned long long g = 0;
        if (lane < 32) g = __hip_atomic_load(sp + lane * CSTR, __ATOMIC_RELAXED, __HIP_MEMORY_SCOPE_AGENT);
        const bool valid = lane < 32 && (unsigned)(g >> 32) == tag0;
        const int cnt = __builtin_popcount((unsigned)__ballot(valid));
        const float sum = wave_sum(valid ? (float)(int)((unsigned)g - tstart) : 0.f);
        if (cnt >= 24) lds_st(&sm.ctl[OFFW], ((tstart + (int)(sum * __builtin_amdgcn_rcpf((float)cnt)) + TOFF) & ~3u) | 1u);
      }
    }
    wait4(&sm.ctl[QFLAG]);
    v4u qf[4];
    {
      float q0 = 0.f, q1 = 0.f;
#pragma unroll
      for (int j = 0; j < 4; ++j) { const v2f p = *(const v2f*)&sm.qpart[j][2 * lane]; q0 += p[0]; q1 += p[1]; }
      sm.qbf[ci][lane] = cvtpk(q0, q1);
      asm volatile("s_waitcnt lgkmcnt(0)" ::: "memory");
#pragma unroll
      for (int s = 0; s < 4; ++s) qf[s] = *(const v4u*)&sm.qbf[ci][16 * s + 4 * t];
    }
    float m = -INFINITY, l = 0.f;
    auto scoreK = [&](const v4u (&Kr)[16], int tile) {
      v4f S[4];
#pragma unroll
      for (int u = 0; u < 4; ++u) S[u] = v4f{0.f, 0.f, 0.f, 0.f};
#pragma unroll
      for (int s = 0; s < 4; ++s)
#pragma unroll
        for (int u = 0; u < 4; ++u) S[u] = mfma16(qf[s], Kr[4 * u + s], S[u]);
      const float sr = t == 0 ? S[0][0] : t == 1 ? S[1][0] : t == 2 ? S[2][0] : S[3][0];
      const unsigned sb = cvtpk(rbf(sr) * SCALE, 0.f);
      sm.sc[64 * tile + lane] = (uint16_t)sb;
      const float sv = bflo(sb);
      const float mn = fmaxf(m, sv);
      l = fmaf(l, expn(m - mn), expn(sv - mn));
      m = mn;
    };
#pragma unroll
    for (int k = 0; k < NPRE; ++k) scoreK(Kp[k], ci + NCONS * k);
    while (n < NKT) {
      v4u Kr[16];
      fetchK(Kr);
      scoreK(Kr, n - NCONS);
    }
    {
      const float mw = wave_max(m);
      const float lw = wave_sum(l * expn(m - mw));
      if (lane == 0) { sm.ml[ci][0] = mw; sm.ml[ci][1] = lw; }
      asm volatile("s_waitcnt lgkmcnt(0)" ::: "memory");
      lds_st(&sm.ctl[KDONE + ci], 1u);
      wait4(&sm.ctl[KDONE]);
    }
    float M = -INFINITY, L = 0.f;
#pragma unroll
    for (int w = 0; w < NCONS; ++w) M = fmaxf(M, sm.ml[w][0]);
#pragma unroll
    for (int w = 0; w < NCONS; ++w) L = fmaf(sm.ml[w][1], expn(sm.ml[w][0] - M), L);
    const float invL = 1.0f / L;
    const unsigned tag = sm.ctl[TAG];
    v4f O0 = {0.f, 0.f, 0.f, 0.f}, O1 = {0.f, 0.f, 0.f, 0.f};
    const unsigned vof = t * 1024 + c * 16;
    auto probs = [&](int tile, unsigned (&pd)[16]) {
#pragma unroll
      for (int qd = 0; qd < 4; ++qd) {
        const v2u sv = *(const v2u*)&sm.sc[64 * (tile - NKT) + 16 * qd + 4 * t];
#pragma unroll
        for (int i = 0; i < 2; ++i) {
          const float p0 = expn(bflo(sv[i]) - M) * invL, p1 = expn(bfhi(sv[i]) - M) * invL;
          pd[4 * qd + 2 * i] = cvtpk(p0, p0); pd[4 * qd + 2 * i + 1] = cvtpk(p1, p1);
        }
      }
    };
    const int nend = ci == LASTC ? NTILE - 1 - NCONS : NTILE - 1;
    while (n < nend) {
      const unsigned char* tb = sm.ring + slot * TILE_B + vof;
      unsigned pd[16];
      probs(n, pd);
#pragma unroll
      for (int hf = 0; hf < 2; ++hf) {
        v4u pa[8];
#pragma unroll
        for (int i = 0; i < 8; ++i) pa[i] = diag(pd[8 * hf + i]);
#pragma unroll
        for (int i = 0; i < 8; ++i) asm volatile("" : "+v"(pa[i]));
        while (lds_ld(&sm.ctl[LANDED]) < (unsigned)(2 * n + 1 + hf)) {}
        asm volatile("" ::: "memory");
        v4u Vr[8];
#pragma unroll
        for (int i = 0; i < 4; ++i) { Vr[i] = *(const v4u*)(tb + hf * 8192 + i * 256); Vr[4 + i] = *(const v4u*)(tb + hf * 8192 + 4096 + i * 256); }
#pragma unroll
        for (int i = 0; i < 4; ++i) { O0 = mfma16(pa[i], Vr[i], O0); O1 = mfma16(pa[4 + i], Vr[4 + i], O1); }
      }
      if (ci != LASTC && lane < 32) *(v4f*)&sm.qpart[ci][8 * c + 4 * t] = O0 + O1;
      asm volatile("s_waitcnt lgkmcnt(0)" ::: "memory");
      lds_st(&sm.ctl[FREED + ci], n + 1);
      const int step = n >= NTILE - 2 * NCONS ? NCONS - 1 : NCONS;
      n += step;
      slot += step; slot = slot >= NSLOT ? slot - NSLOT : slot;
    }
    if (ci == LASTC) {
      const char* vp = vc + hb + (NKT - 1) * TILE_B + vof;
      unsigned pd[16];
      probs(NTILE - 1, pd);
      v4u pa[16];
#pragma unroll
      for (int i = 0; i < 16; ++i) pa[i] = diag(pd[i]);
#pragma unroll
      for (int i = 0; i < 16; ++i) asm volatile("" : "+v"(pa[i]));
      while (!lds_ld(&sm.ctl[LGO])) {}
      asm volatile("" : "+v"(vp));
      v4u Vr[16];
#pragma unroll
      for (int i = 0; i < 16; ++i) Vr[i] = ldnt(vp + (i >> 2) * 4096 + (i & 3) * 256);
      __builtin_amdgcn_sched_barrier(0);
      lds_st(&sm.ctl[WGO], 1u);
      const __amdgpu_buffer_rsrc_t ro = __builtin_amdgcn_make_buffer_rsrc(ws + WS_OBOX + (size_t)(h * NB + b) * OBOX_B, 0, OBOX_B, 0x00020000);
      const unsigned oo = lane < 32 ? (2 * c + t) * 16 : OOB;
      __builtin_amdgcn_sched_barrier(0);
#pragma unroll
      for (int i = 0; i < LSPLIT / 2; ++i) { O0 = mfma16(pa[2 * i], Vr[2 * i], O0); O1 = mfma16(pa[2 * i + 1], Vr[2 * i + 1], O1); }
      O0 += O1;
      asm volatile("" : "+v"(O0));
      for (;;) {
        const v4u f = *(const volatile LAS v4u*)&sm.ctl[FREED];
        const v4f u0 = *(const volatile LAS v4f*)&sm.qpart[(LASTC + 1) % NCONS][8 * c + 4 * (t & 1)];
        const v4f u1 = *(const volatile LAS v4f*)&sm.qpart[(LASTC + 2) % NCONS][8 * c + 4 * (t & 1)];
        O1 = u0 + u1;
        if (__builtin_amdgcn_readfirstlane(min(f[(LASTC + 1) % NCONS], f[(LASTC + 2) % NCONS])) >= (unsigned)(NTILE - 2)) break;
      }
      O0 += O1;
      asm volatile("" : "+v"(O0));
      __builtin_amdgcn_sched_barrier(0);
#pragma unroll
      for (int i = LSPLIT; i < 16; ++i) O0 = mfma16(pa[i], Vr[i], O0);
      const v4u g = {cvtpk(O0[0], O0[1]), tag, cvtpk(O0[2], O0[3]), tag};
      __builtin_amdgcn_raw_buffer_store_b128(g, ro, oo, 0, 16);
    }
    __builtin_amdgcn_s_waitcnt(0x0F70);
    __builtin_amdgcn_endpgm();
  } else {
    const int j = wave - 4;
    const uint16_t* xr = x + b * DM;
    const v4u xa = ldg(xr + 8 * lane), xb = ldg(xr + 512 + 8 * c);
    float m1, den1, xv[16];
    unsigned tag;
    {
      v4u xq[5];
#pragma unroll
      for (int n = 0; n < 5; ++n) xq[n] = ldg(xr + 160 * j + 40 * t + 8 * n);
      v4u W[40];
      const char* p = wq + (size_t)(160 * j + 40 * t) * (NH * DH * 2) + h * (DH * 2) + c * 16;
      v4f Q0 = {0.f, 0.f, 0.f, 0.f}, Q1 = {0.f, 0.f, 0.f, 0.f};
      auto pair = [&](int k) {
        const unsigned w = xq[k >> 2][k & 3];
        const float e0 = rbf(bflo(w) - m1) * den1, e1 = rbf(bfhi(w) - m1) * den1;
        Q0 = mfma16(diag(cvtpk(e0, e0)), W[2 * k], Q0);
        Q1 = mfma16(diag(cvtpk(e1, e1)), W[2 * k + 1], Q1);
      };
      int done = 0;
#pragma unroll
      for (int i = 0; i < 40; ++i) {
        if (i == WQD) wg_barrier();
        if (i >= WQD) { asm volatile("s_waitcnt vmcnt(%0)" :: "i"(WQD - 1) : "memory"); __builtin_amdgcn_sched_barrier(0); }
        W[i] = ldnt(p + (size_t)i * (NH * DH * 2));
        __builtin_amdgcn_sched_barrier(0);
        if (i == LNAT) {
#pragma unroll
          for (int k = 0; k < 4; ++k) { xv[2 * k] = bflo(xa[k]); xv[2 * k + 1] = bfhi(xa[k]); xv[8 + 2 * k] = bflo(xb[k]); xv[9 + 2 * k] = bfhi(xb[k]); }
          ln_stats<16>(xv, lane < 16, m1, den1);
          __builtin_amdgcn_sched_barrier(0);
        }
        if (i > LNAT && 2 * done + 1 <= i - WQD) { pair(done); ++done; __builtin_amdgcn_sched_barrier(0); }
      }
#pragma unroll
      for (int k = 0; k < 20; ++k) if (k >= done) pair(k);
      Q0 += Q1;
      if (lane < 32) *(v4f*)&sm.qpart[j][8 * c + 4 * t] = Q0;
      asm volatile("s_waitcnt lgkmcnt(0)" ::: "memory");
      lds_st(&sm.ctl[QFLAG + j], 1u);
    }
    while (!lds_ld(&sm.ctl[TAGOK])) __builtin_amdgcn_s_sleep(1);
    tag = lds_ld(&sm.ctl[TAG]);
    unsigned org0 = 0, orgn = 0;
    float slope = 0.f;
    auto follow = [&](unsigned upto) {
      for (;;) {
        const unsigned ld = lds_ld(&sm.ctl[LANDED]);
        if (ld >= upto) break;
        if (orgn) lds_st(&sm.ctl[OFFW], (org0 + (int)(slope * (float)(int)(ld - orgn))) | 3u);
        __builtin_amdgcn_s_sleep(4);
      }
    };
    auto calibrate = [&](int r, int tile0) {
      const int sx = bid & 7, sy = bid >> 3;
      const bool contrib = ((sy + sx) & 7) == 1;
      GAS unsigned long long* mp = (GAS unsigned long long*)(ws + WS_CAL + r * 4096);
      const unsigned n0 = 2 * tile0;
      follow(n0);
      const unsigned eo = lds_ld(&sm.ctl[EOWN]), b0 = lds_ld(&sm.ctl[BIAS]);
      if (contrib && lane == 0) __hip_atomic_store(mp + (sx * 4 + (sy >> 3)) * CSTR, ((unsigned long long)tag << 32) | eo, __ATOMIC_RELAXED, __HIP_MEMORY_SCOPE_AGENT);
      follow(n0 + RDLY);
      unsigned long long g = 0;
      if (lane < 32) g = __hip_atomic_load(mp + lane * CSTR, __ATOMIC_RELAXED, __HIP_MEMORY_SCOPE_AGENT);
      const bool valid = lane < 32 && (unsigned)(g >> 32) == tag;
      const int cnt = __builtin_popcount((unsigned)__ballot(valid));
      const float sum = wave_sum(valid ? (float)(int)((unsigned)g - eo) : 0.f);
      if (cnt >= 20) {
        const unsigned org = eo + (int)(sum * __builtin_amdgcn_rcpf((float)cnt)) - CEN - b0;
        if (orgn) slope = fminf(fmaxf((float)(int)(org - org0) * __builtin_amdgcn_rcpf((float)(n0 - orgn)), -0.5f), 6.f);
        org0 = org; orgn = n0;
        lds_st(&sm.ctl[OFFW], org | 3u);
      }
    };
    const bool two = (h & 3) == 0;
    const int gcol = lane < 16 ? 16 * h + lane : 512 + 4 * h + c;
    unsigned xcol = 0, bb = 0;
    {
      const int fo = 8 * (54 * j + (lane < 54 ? lane : 0));
      const v4u r1 = ldg(w1 + fo), r2 = ldg(b1 + fo);
      v4u r3 = ldg(w2 + fo);
      xcol = xr[two ? gcol : 16 * h + c];
      bb = b2[0];
      r3 = lane < 54 ? r3 : v4u{0u, 0u, 0u, 0u};
      float gw1[8], gb1[8], gw2[8];
#pragma unroll
      for (int i = 0; i < 4; ++i) {
        gw1[2 * i] = bflo(r1[i]); gw1[2 * i + 1] = bfhi(r1[i]); gb1[2 * i] = bflo(r2[i]); gb1[2 * i + 1] = bfhi(r2[i]);
        gw2[2 * i] = bflo(r3[i]); gw2[2 * i + 1] = bfhi(r3[i]);
      }
      float m2, den2;
#pragma unroll
      for (int i = 0; i < 16; ++i) xv[i] = ln_apply(xv[i], m1, den1);
      ln_stats<16>(xv, lane < 16, m2, den2);
      const float hv = ln_apply(ln_apply(bflo(xcol), m1, den1), m2, den2);
      float gsum = 0.f;
      const int ncol = two ? 32 : 16;
#pragma unroll 2
      for (int col = 0; col < ncol; ++col) {
        const float hc = rdlane(hv, col);
        float acc = 0.f;
#pragma unroll
        for (int i = 0; i < 8; ++i) {
          const float u = rbf(rbf(hc * gw1[i]) + gb1[i]);
          const float z = u * fmaf(u * u, 0.044715f * 0.7978845608028654f * 2.f * LOG2E, 0.7978845608028654f * 2.f * LOG2E);
          const float ge = rbf(u * __builtin_amdgcn_rcpf(1.f + __builtin_amdgcn_exp2f(-z)));
          acc = fmaf(ge, gw2[i], acc);
        }
        acc = wave_sum(acc);
        gsum = lane == col ? acc : gsum;
      }
      if (lane < 32) sm.gpart[j][lane] = gsum;
    }
    asm volatile("s_waitcnt lgkmcnt(0)" ::: "memory");
    lds_st(&sm.ctl[GDONE + j], 1u);
    if (j == 1) {
#pragma nounroll
      for (int r = 0; r < NRND; ++r) calibrate(r, RN0 + r * RNS);
      follow(2 * NTILE - 8);
    }
    if (j < NG) {
      constexpr int NT = (5 + NG - 1) / NG;
      const v4i ro = mkrsrc(ws + WS_OBOX + (size_t)h * (NB * OBOX_B), NB * OBOX_B);
      const __amdgpu_buffer_rsrc_t rs = __builtin_amdgcn_make_buffer_rsrc(ws + WS_SBOX, 0, NB * SBOX_B, 0x00020000);
      const unsigned go = (c & 7) * OBOX_B + (2 * t + (c >> 3)) * 16;
      const unsigned so = (((c & 7) * 40 + 5 * b) * NH + h) * 128 + t * 32 + (c >> 3) * 16;
      while (!lds_ld(&sm.ctl[WGO])) __builtin_amdgcn_s_sleep(2);
      for (;;) {
        const v2u wf = *(const volatile LAS v2u*)&sm.ctl[WLAND];
        if (__builtin_amdgcn_readfirstlane(wf[0] & wf[1])) break;
      }
      asm volatile("" ::: "memory");
      v4u Wt[NT][4];
      const unsigned char* wl = sm.ring + WSLOT * TILE_B + (8 * t + (c >> 2)) * 160 + (c & 3) * 8;
#pragma unroll
      for (int e = 0; e < NT; ++e)
#pragma unroll
        for (int s = 0; s < 4; ++s) {
          const int ct = j + NG * e < 5 ? j + NG * e : 0;
          const v2u lo = __builtin_bit_cast(v2u, __builtin_amdgcn_ds_read_tr16_b64_v4i16((LAS v4s*)(wl + 32 * s * 160 + 32 * ct)));
          const v2u hi = __builtin_bit_cast(v2u, __builtin_amdgcn_ds_read_tr16_b64_v4i16((LAS v4s*)(wl + (32 * s + 4) * 160 + 32 * ct)));
          Wt[e][s] = v4u{lo[0], lo[1], hi[0], hi[1]};
        }
#pragma unroll
      for (int e = 0; e < NT; ++e)
#pragma unroll
        for (int s = 0; s < 4; ++s) asm volatile("" : "+v"(Wt[e][s]));
      v4u r[4];
      gather4<128>(r, go, ro, tag);
      v4f D[NT];
#pragma unroll
      for (int e = 0; e < NT; ++e) D[e] = v4f{0.f, 0.f, 0.f, 0.f};
#pragma unroll
      for (int s = 0; s < 4; ++s) {
        const unsigned y0 = __builtin_amdgcn_mov_dpp(r[s][0], ROR8, 0xf, 0xf, true), y1 = __builtin_amdgcn_mov_dpp(r[s][2], ROR8, 0xf, 0xf, true);
        const v4u ob = c < 8 ? v4u{r[s][0], r[s][2], y0, y1} : v4u{y0, y1, r[s][0], r[s][2]};
#pragma unroll
        for (int e = 0; e < NT; ++e) D[e] = mfma16(Wt[e][s], ob, D[e]);
      }
#pragma unroll
      for (int e = 0; e < NT; ++e) {
        const v4u g = {__float_as_uint(c < 8 ? D[e][0] : D[e][2]), tag, __float_as_uint(c < 8 ? D[e][1] : D[e][3]), tag};
        __builtin_amdgcn_raw_buffer_store_b128(g, rs, j + NG * e < 5 ? so + (j + NG * e) * (NH * 128) : OOB, 0, 16);
      }
      lds_st(&sm.ctl[SENT + j], 1u);
    } else if (j >= 2) {
      wait4(&sm.ctl[GDONE]);
      const int e = 3 - j, ct = e ? 32 + (h >> 2) : h, cp = lane & 7;
      float y0 = 0.f, y1 = 0.f;
#pragma unroll
      for (int w = 0; w < 4; ++w) { y0 += sm.gpart[w][16 * e + 2 * cp]; y1 += sm.gpart[w][16 * e + 2 * cp + 1]; }
      y0 = rbf(rbf(y0) + bflo(bb)); y1 = rbf(rbf(y1) + bflo(bb));
      float g0 = rbf(y0 / (1.f + expn(-y0))), g1 = rbf(y1 / (1.f + expn(-y1)));
      float x0 = bflo(__shfl(xcol, 16 * e + 2 * cp)), x1 = bflo(__shfl(xcol, 16 * e + 2 * cp + 1));
      asm volatile("" : "+v"(g0), "+v"(g1), "+v"(x0), "+v"(x1));
      const v4i rs = mkrsrc(ws + WS_SBOX + (size_t)(b * 40 + ct) * (NH * 128), NH * 128);
      const __amdgpu_buffer_rsrc_t rout = __builtin_amdgcn_make_buffer_rsrc(out, 0, NB * DM * 2, 0x00020000);
      unsigned wof[5];
#pragma unroll
      for (int p = 0; p < 5; ++p) {
        const unsigned id = 64 * p + lane, row = id / 10;
        wof[p] = row * (DM * 2) + (id - 10 * row) * 16;
        asm volatile("" : "+v"(wof[p]));
      }
      const char* wb = wo + (size_t)(DH * h + e * 64) * (DM * 2) + b * 160;
      const unsigned wdst = (unsigned)(size_t)(LAS unsigned char*)sm.ring + WSLOT * TILE_B + e * 64 * 160;
      while (lds_ld(&sm.ctl[FREED + (NTILE - NSLOT) % NCONS]) < (unsigned)(NTILE - NSLOT + 1) ||
             lds_ld(&sm.ctl[FREED + (NTILE - NSLOT + 1) % NCONS]) < (unsigned)(NTILE - NSLOT + 2)) __builtin_amdgcn_s_sleep(2);
      while (!lds_ld(&sm.ctl[WGO])) {}
#pragma unroll
      for (int r = 0; r < 2; ++r)
#pragma unroll
        for (int p = 0; p < 5; ++p) dma1(wdst + (5 * r + p) * 1024, wb + r * 32 * (DM * 2), wof[p]);
      asm volatile("s_waitcnt vmcnt(0)" ::: "memory");
      lds_st(&sm.ctl[WLAND + e], 1u);
      if (!e || two) {
        wait4(&sm.ctl[SENT]);
        __builtin_amdgcn_s_sleep(S2DELAY);
        v4u r[4];
        gather4<1024>(r, lane * 16, rs, tag);
        if (bid == 0 && tid == 448) __hip_atomic_store(epoch_p, tag, __ATOMIC_RELAXED, __HIP_MEMORY_SCOPE_AGENT);
        float s0 = (__uint_as_float(r[0][0]) + __uint_as_float(r[1][0])) + (__uint_as_float(r[2][0]) + __uint_as_float(r[3][0]));
        float s1 = (__uint_as_float(r[0][2]) + __uint_as_float(r[1][2])) + (__uint_as_float(r[2][2]) + __uint_as_float(r[3][2]));
        s0 += dpp<ROR8>(s0); s1 += dpp<ROR8>(s1);
        s0 = rbf(swap32_sum(swap16_sum(s0))); s1 = rbf(swap32_sum(swap16_sum(s1)));
        const unsigned res = cvtpk(x0 + rbf(s0 * g0), x1 + rbf(s1 * g1));
        __builtin_amdgcn_raw_buffer_store_b32(res, rout, lane < 8 ? (b * DM + 16 * ct + 2 * cp) * 2 : OOB, 0, 16);
      }
    }
    __builtin_amdgcn_s_waitcnt(0x0F70);
    __builtin_amdgcn_endpgm();
  }
}

extern "C" void kernel_launch(void* const* d_in, const int* in_sizes, int n_in, void* d_out, int out_size, void* d_ws,
                              size_t ws_size, hipStream_t stream) {
  fused_decode<<<256, 512, 0, stream>>>((const uint16_t*)d_in[0], (const char*)d_in[1], (const char*)d_in[2],
                                         (const char*)d_in[3], (const char*)d_in[4], (const uint16_t*)d_in[6],
                                         (const uint16_t*)d_in[7], (const uint16_t*)d_in[8], (const uint16_t*)d_in[9],
                                         (uint16_t*)d_out, (char*)d_ws);
}
```
